```python
import torch

def _softmax(x, axis=-1):
    return torch.softmax(x, dim=axis)
def _gelu(x):
    return torch.nn.functional.gelu(x, approximate="tanh")
def _silu(x):
    return torch.nn.functional.silu(x)
def _sigmoid(x):
    return torch.sigmoid(x)
def _layernorm(x, axis=-1, eps=1e-5):
    m = x.mean(dim=axis, keepdim=True)
    v = x.var(dim=axis, keepdim=True, unbiased=False)
    return (x - m) / torch.sqrt(v + eps)
def _scatter_add(tgt, idx, src, axis=0):
    return tgt.clone().index_add_(axis, idx, src)
def _segment_sum(src, seg, n):
    out = torch.zeros((n,) + src.shape[1:], dtype=src.dtype)
    return out.index_add_(0, seg, src)
def _pad(x, pw):
    flat = tuple(v for pair in reversed(pw) for v in pair)
    return torch.nn.functional.pad(x, flat)

kernel_name = 'concat_heads__ssm_conv_s4d__ssm_conv_s4__prompt'

def setup_inputs():

    _w = (lambda a: (lambda t: t.to(torch.bfloat16) if t.is_floating_point() else t)(torch.as_tensor(a)))
    def _A_setup():
        Bsz, L, D, N = 1, 2048, 640, 192
        return {
            "u": _w((lambda *s: torch.randn(*s, dtype=torch.bfloat16) * 0.05)(Bsz, L, D)),
            "log_A_real": _w((lambda *s: torch.randn(*s, dtype=torch.bfloat16) * 6.4)(D, N) * 0.5),
            "A_imag": _w((lambda *s: torch.randn(*s, dtype=torch.bfloat16) * 0.05)(D, N) * 3.14),
            "C_real": _w((lambda *s: torch.randn(*s, dtype=torch.bfloat16) * 0.05)(D, N) / N ** 0.5),
            "C_imag": _w((lambda *s: torch.randn(*s, dtype=torch.bfloat16) * 0.05)(D, N) / N ** 0.5),
            "log_dt": _w((lambda *s: torch.randn(*s, dtype=torch.bfloat16) * 0.05)(D) * 0.5 - 4.0),
            "D_skip": _w((lambda *s: torch.randn(*s, dtype=torch.bfloat16) * 0.05)(D)),
        }
    _A_in = _A_setup()

    def _B_setup():
        B, L, D, N = 1, 2048, 640, 192
        A_re = -(lambda _x: torch.exp(torch.as_tensor(_x)))((lambda *s: torch.randn(*s, dtype=torch.bfloat16) * 6.4)(D, N))
        A_im = torch.pi * (lambda *s: torch.randn(*s, dtype=torch.bfloat16) * 0.05)(D, N)
        B_re = (lambda *s: torch.randn(*s, dtype=torch.bfloat16) * 0.05)(D, N)
        B_im = (lambda *s: torch.randn(*s, dtype=torch.bfloat16) * 0.05)(D, N)
        C_re = (lambda *s: torch.randn(*s, dtype=torch.bfloat16) * 0.05)(D, N)
        C_im = (lambda *s: torch.randn(*s, dtype=torch.bfloat16) * 0.05)(D, N)
        log_dt = (lambda *s: torch.randn(*s, dtype=torch.bfloat16) * 0.05)(D) * 0.5 - 4.0
        Dskip = (lambda *s: torch.randn(*s, dtype=torch.bfloat16) * 0.05)(D)
        return {
            "u": _w((lambda *s: torch.randn(*s, dtype=torch.bfloat16) * 0.05)(B, L, D)),
            "A_re": _w(A_re),
            "A_im": _w(A_im),
            "B_re": _w(B_re),
            "B_im": _w(B_im),
            "C_re": _w(C_re),
            "C_im": _w(C_im),
            "log_dt": _w(log_dt),
            "Dskip": _w(Dskip),
        }
    _B_in = _B_setup()

    return {
        "A_u": _A_in["u"],
        "A_log_A_real": _A_in["log_A_real"],
        "A_A_imag": _A_in["A_imag"],
        "A_C_real": _A_in["C_real"],
        "A_C_imag": _A_in["C_imag"],
        "A_log_dt": _A_in["log_dt"],
        "A_D_skip": _A_in["D_skip"],
        "B_u": _B_in["u"],
        "B_A_re": _B_in["A_re"],
        "B_A_im": _B_in["A_im"],
        "B_B_re": _B_in["B_re"],
        "B_B_im": _B_in["B_im"],
        "B_C_re": _B_in["C_re"],
        "B_C_im": _B_in["C_im"],
        "B_log_dt": _B_in["log_dt"],
        "B_Dskip": _B_in["Dskip"],
    }

def reference(A_u, A_log_A_real, A_A_imag, A_C_real, A_C_imag, A_log_dt, A_D_skip, B_u, B_A_re, B_A_im, B_B_re, B_B_im, B_C_re, B_C_im, B_log_dt, B_Dskip):
    def _A_ref(u, log_A_real, A_imag, C_real, C_imag, log_dt, D_skip):
        L = u.shape[1]
        dt = torch.exp(log_dt)
        A = -torch.exp(log_A_real) + 1j * A_imag
        C = (C_real + 1j * C_imag) * dt[:, None]
        pos = (lambda x, dt: x.to(dt))(torch.arange(L), log_dt.dtype)
        P = torch.exp((A * dt[:, None])[:, :, None] * pos[None, None, :])
        K = 2.0 * torch.einsum("dn,dnl->dl", C, P).real
        L2 = 2 * L
        u_t = (lambda x, *d: x.permute(*d))(u, 0, 2, 1)
        Kf = (lambda x, *a, **k: torch.fft.rfft(x.float(), *a, **k))(K, L2)
        Uf = (lambda x, *a, **k: torch.fft.rfft(x.float(), *a, **k))(u_t, L2)
        y = (lambda x, *a, **k: torch.fft.irfft(x, *a, **k).to(torch.bfloat16))(Uf * Kf[None, :, :], L2)[:, :, :L]
        y = (lambda x, *d: x.permute(*d))(y, 0, 2, 1)
        return y + u * D_skip[None, None, :]

    def _B_ref(u, A_re, A_im, B_re, B_im, C_re, C_im, log_dt, Dskip):
        Bsz, L, D = u.shape
        dt = torch.exp(log_dt)[:, None]
        A = A_re + 1j * A_im
        Bc = B_re + 1j * B_im
        Cc = C_re + 1j * C_im
        logdA = dt * A
        dB = dt * Bc
        l = (lambda x, dt: x.to(dt))(torch.arange(L), A_re.dtype)
        P = torch.exp(logdA[:, :, None] * l[None, None, :])
        K = torch.einsum("dn,dnl,dn->dl", Cc, P, dB).real
        L2 = 2 * L
        Kp = _pad(K, ((0, 0), (0, L)))
        up = (lambda x, *d: x.permute(*d))(u, 0, 2, 1)
        up = _pad(up, ((0, 0), (0, 0), (0, L)))
        Kf = (lambda x, axes=(-2,-1): torch.fft.rfft2(x.float(), dim=axes))(Kp[:, None, :], axes=(-2, -1))
        Uf = (lambda x, axes=(-2,-1): torch.fft.rfft2(x.float(), dim=axes))(up[:, :, None, :], axes=(-2, -1))
        Yf = Uf * Kf[None, :, :, :]
        y = (lambda x, s, axes=(-2,-1): torch.fft.irfft2(x, s=s, dim=axes).to(torch.bfloat16))(Yf, (1, L2), axes=(-2, -1))[:, :, 0, :L]
        y = (lambda x, *d: x.permute(*d))(y, 0, 2, 1) + u * Dskip[None, None, :]
        return y

    a = _A_ref(A_u, A_log_A_real, A_A_imag, A_C_real, A_C_imag, A_log_dt, A_D_skip)
    b = _B_ref(B_u, B_A_re, B_A_im, B_B_re, B_B_im, B_C_re, B_C_im, B_log_dt, B_Dskip)
    return torch.concatenate([a, b], axis=-1)
```

```cpp
#include <hip/hip_runtime.h>
#include <hip/hip_bf16.h>
#include <stdint.h>

#define L_ 2048
#define D_ 640
#define N_ 192

__device__ __forceinline__ float bf2f(uint16_t x){ return __uint_as_float(((uint32_t)x)<<16); }
__device__ __forceinline__ uint16_t f2bf(float f){ uint32_t u=__float_as_uint(f); u += 0x7fffu + ((u>>16)&1u); return (uint16_t)(u>>16);}
__device__ __forceinline__ float rbf(float f){ return bf2f(f2bf(f)); }

__global__ void kgen(const uint16_t* A_logA, const uint16_t* A_im, const uint16_t* A_cr, const uint16_t* A_ci, const uint16_t* A_ldt,
                     const uint16_t* B_are, const uint16_t* B_aim, const uint16_t* B_bre, const uint16_t* B_bim, const uint16_t* B_cre, const uint16_t* B_cim, const uint16_t* B_ldt,
                     float* K){
  int d = blockIdx.y; int l = blockIdx.x*256+threadIdx.x;
  float pos = rbf((float)l);
  float acc=0.f;
  if (d < D_) {
    float dt = rbf(expf(bf2f(A_ldt[d])));
    for (int n=0;n<N_;n++){
      float ar = -rbf(expf(bf2f(A_logA[d*N_+n]))); float ai = bf2f(A_im[d*N_+n]);
      float cr = bf2f(A_cr[d*N_+n])*dt, ci = bf2f(A_ci[d*N_+n])*dt;
      float er = ar*dt*pos, ei = (ai*dt)*pos;
      float m = expf(er); float s,c; sincosf(ei,&s,&c);
      acc += cr*(m*c) - ci*(m*s);
    }
    acc *= 2.f;
  } else {
    int e = d - D_;
    float dt = rbf(expf(bf2f(B_ldt[e])));
    for (int n=0;n<N_;n++){
      float ar = bf2f(B_are[e*N_+n]); float ai = bf2f(B_aim[e*N_+n]);
      float br = bf2f(B_bre[e*N_+n])*dt, bi = bf2f(B_bim[e*N_+n])*dt;
      float cr0 = bf2f(B_cre[e*N_+n]), ci0 = bf2f(B_cim[e*N_+n]);
      float cr = cr0*br - ci0*bi, ci = cr0*bi + ci0*br;
      float er = ar*dt*pos, ei = (ai*dt)*pos;
      float m = expf(er); float s,c; sincosf(ei,&s,&c);
      acc += cr*(m*c) - ci*(m*s);
    }
  }
  K[d*L_+l]=acc;
}

__global__ void conv(const uint16_t* uA, const uint16_t* uB, const uint16_t* DA, const uint16_t* DB, const float* K, uint16_t* out){
  int d = blockIdx.x*64 + (threadIdx.x&63); int t = blockIdx.y*4 + (threadIdx.x>>6);
  const uint16_t* u = d < D_ ? uA : uB; int e = d < D_ ? d : d - D_;
  float Dk = bf2f(d < D_ ? DA[e] : DB[e]);
  float acc=0.f;
  for (int j=0;j<=t;j++) acc += K[d*L_+j]*bf2f(u[(t-j)*D_+e]);
  float y = rbf(acc);
  float s = rbf(bf2f(u[t*D_+e])*Dk);
  out[t*2*D_+d] = f2bf(y+s);
}

extern "C" void kernel_launch(void* const* d_in, const int* in_sizes, int n_in,
                              void* d_out, int out_size, void* d_ws, size_t ws_size,
                              hipStream_t stream) {
  const uint16_t** in = (const uint16_t**)d_in;
  float* K = (float*)d_ws;
  kgen<<<dim3(L_/256, 2*D_), 256, 0, stream>>>(in[1],in[2],in[3],in[4],in[5], in[8],in[9],in[10],in[11],in[12],in[13],in[14], K);
  conv<<<dim3(2*D_/64, L_/4), 256, 0, stream>>>(in[0], in[7], in[6], in[15], K, (uint16_t*)d_out);
}
```

```cpp
#include <hip/hip_runtime.h>
#include <hip/hip_bf16.h>
#include <stdint.h>

typedef float f2 __attribute__((ext_vector_type(2)));
typedef float f4 __attribute__((ext_vector_type(4)));
typedef __bf16 bf2 __attribute__((ext_vector_type(2)));
typedef __bf16 bf8 __attribute__((ext_vector_type(8)));
typedef unsigned u2 __attribute__((ext_vector_type(2)));
typedef unsigned u3 __attribute__((ext_vector_type(3)));
typedef unsigned u4 __attribute__((ext_vector_type(4)));
#define DI __device__ __forceinline__
#define LDSP(T, off) ((__attribute__((address_space(3))) T*)(unsigned long)(unsigned)(off))

#define STAMP(i)

constexpr int NMODE = 192;
constexpr int UT_PAD = 288;
constexpr int UTS = (UT_PAD + 2048) * 2;
constexpr int OFF_UT = 0;
constexpr int OFF_RX = 6 * UTS;
constexpr int SCR = 16 * 288;
constexpr int KNC = 1216;
constexpr int OFF_KN = OFF_RX;
constexpr int OFF_KF = OFF_RX + 6 * 4 * KNC;
constexpr int KFS = 896;
constexpr int OFF_YS = OFF_KF + 6 * KFS;
constexpr int OFF_RY = OFF_RX + 12 * SCR;
constexpr int LDS_BYTES = OFF_RY + 18 * 3072;
static_assert(OFF_YS + 12288 <= OFF_RY, "lds alias");

DI f2 cmul(f2 a, f2 b) {
  f2 r;
  asm("v_pk_mul_f32 %0, %1, %2 op_sel_hi:[0,1]\n\t"
      "v_pk_fma_f32 %0, %1, %2, %0 op_sel:[1,1,0] op_sel_hi:[1,0,1] neg_lo:[0,1,0]"
      : "=&v"(r) : "v"(a), "v"(b));
  return r;
}
DI f2 cmulc(f2 a, f2 b) {
  f2 r;
  asm("v_pk_mul_f32 %0, %1, %2 op_sel_hi:[0,1] neg_hi:[0,1]\n\t"
      "v_pk_fma_f32 %0, %1, %2, %0 op_sel:[1,1,0] op_sel_hi:[1,0,1]"
      : "=&v"(r) : "v"(a), "v"(b));
  return r;
}
DI unsigned pk2(float lo, float hi) {
  bf2 r = __builtin_convertvector((f2{lo, hi}), bf2);
  return __builtin_bit_cast(unsigned, r);
}
DI unsigned pk2(f2 a) { return pk2(a.x, a.y); }
DI float bf2f(unsigned short x) { return __uint_as_float(((unsigned)x) << 16); }
DI float rbf(float f) { return __uint_as_float(pk2(f, 0.f) << 16); }
DI f4 mfma(u4 a, u4 b, f4 c) {
  return __builtin_amdgcn_mfma_f32_16x16x32_bf16(__builtin_bit_cast(bf8, a), __builtin_bit_cast(bf8, b), c, 0, 0, 0);
}
DI void bar() { asm volatile("s_waitcnt lgkmcnt(0)\n\ts_barrier" ::: "memory"); }
template <int R0>
DI void wr4(int m0v, unsigned a, unsigned b, unsigned c, unsigned d) {
  asm volatile("s_mov_b32 m0, %0\n\ts_nop 0\n\t"
               "ds_write_addtid_b32 %1 offset:%5\n\tds_write_addtid_b32 %2 offset:%6\n\t"
               "ds_write_addtid_b32 %3 offset:%7\n\tds_write_addtid_b32 %4 offset:%8"
               :: "s"(m0v), "v"(a), "v"(b), "v"(c), "v"(d),
                  "i"(R0 * 288), "i"((R0 + 1) * 288), "i"((R0 + 2) * 288), "i"((R0 + 3) * 288) : "memory");
}
DI f2 cexp_dir(float a2, float b2, float p) {
  float m = __builtin_amdgcn_exp2f(a2 * p);
  float ph = __builtin_amdgcn_fractf(b2 * p);
  return f2{m * __builtin_amdgcn_cosf(ph), m * __builtin_amdgcn_sinf(ph)};
}
DI float nextlane(float v) {
  return __int_as_float(__builtin_amdgcn_update_dpp(0, __float_as_int(v), 0x130, 0xf, 0xf, true));
}
DI int pslot(int g, int a) { return 16 * (16 * g + ((a + 4 * g) & 15)); }

struct Params {
  const unsigned short* u[2];
  const unsigned short* a_re[2];
  const unsigned short* a_im[2];
  const unsigned short* c_re[2];
  const unsigned short* c_im[2];
  const unsigned short* ldt[2];
  const unsigned short* dsk[2];
  const unsigned short* b_re;
  const unsigned short* b_im;
  unsigned short* out;
  unsigned long long* dbg;
};

struct Raw { unsigned short ar, ai, cr, ci, br, bi, ldt; };

DI Raw load_raw(const Params& p, int br, int e, int chunk, int lane) {
  Raw r;
  int idx = e * NMODE + chunk * 64 + lane;
  r.ar = p.a_re[br][idx]; r.ai = p.a_im[br][idx]; r.cr = p.c_re[br][idx]; r.ci = p.c_im[br][idx];
  r.br = 0; r.bi = 0;
  if (br) { r.br = p.b_re[idx]; r.bi = p.b_im[idx]; }
  r.ldt = p.ldt[br][e];
  return r;
}

DI void read4(int ra, u4 (&f)[4]) {
#pragma unroll
  for (int s = 0; s < 4; s++) f[s] = *LDSP(u4, ra + 64 * s);
}

DI void kgen_task(const Raw& r, int br, int scr, int part, int lane) {
  float dt = rbf(expf(bf2f(r.ldt)));
  float ar, cx, cy;
  if (br == 0) {
    ar = -rbf(__builtin_amdgcn_exp2f(bf2f(r.ar) * 1.44269504088896341f));
    cx = 2.f * (bf2f(r.cr) * dt); cy = 2.f * (bf2f(r.ci) * dt);
  } else {
    ar = bf2f(r.ar);
    float bx = bf2f(r.br) * dt, by = bf2f(r.bi) * dt, c0 = bf2f(r.cr), c1 = bf2f(r.ci);
    cx = c0 * bx - c1 * by; cy = c0 * by + c1 * bx;
  }
  float a2 = (ar * dt) * 1.44269504088896341f;
  float b2 = (bf2f(r.ai) * dt) * 0.15915494309189535f;
  const int ra = scr + (lane & 15) * 288 + (lane >> 4) * 16;
  u4 fa[4], fx[4];
  f4 z4v = {0.f, 0.f, 0.f, 0.f};
  f4 d0 = z4v, d1 = z4v, d2 = z4v;
  f2 z1 = cexp_dir(a2, b2, 1.f);
  f2 z256 = cexp_dir(a2, b2, 256.f);
  f2 z2 = cmul(z1, z1), z3 = cmul(z2, z1), z4 = cmul(z2, z2);
  wr4<0>(scr, 0x00003f80u, pk2(z1), pk2(z2), pk2(z3));
  f2 z5 = cmul(z4, z1), z6 = cmul(z4, z2), z7 = cmul(z4, z3), z8 = cmul(z4, z4);
  wr4<4>(scr, pk2(z4), pk2(z5), pk2(z6), pk2(z7));
  f2 z9 = cmul(z8, z1), z10 = cmul(z8, z2), z11 = cmul(z8, z3), z12 = cmul(z8, z4);
  wr4<8>(scr, pk2(z8), pk2(z9), pk2(z10), pk2(z11));
  f2 z13 = cmul(z8, z5), z14 = cmul(z8, z6), z15 = cmul(z8, z7), w = cmul(z8, z8);
  wr4<12>(scr, pk2(z12), pk2(z13), pk2(z14), pk2(z15));
  read4(ra, fa);
  f2 w2 = cmul(w, w);
  f2 x0 = f2{cx, -cy};
  f2 x1 = cmulc(x0, w);
  f2 x2 = cmulc(x0, w2), x3 = cmulc(x1, w2);
  wr4<0>(scr, pk2(x0), pk2(x1), pk2(x2), pk2(x3));
  x0 = cmulc(x2, w2); x1 = cmulc(x3, w2); x2 = cmulc(x0, w2); x3 = cmulc(x1, w2);
  wr4<4>(scr, pk2(x0), pk2(x1), pk2(x2), pk2(x3));
  x0 = cmulc(x2, w2); x1 = cmulc(x3, w2); x2 = cmulc(x0, w2); x3 = cmulc(x1, w2);
  wr4<8>(scr, pk2(x0), pk2(x1), pk2(x2), pk2(x3));
  x0 = cmulc(x2, w2); x1 = cmulc(x3, w2); x2 = cmulc(x0, w2); x3 = cmulc(x1, w2);
  wr4<12>(scr, pk2(x0), pk2(x1), pk2(x2), pk2(x3));
  read4(ra, fx);
  f2 z512 = cmul(z256, z256);
  wr4<0>(scr, pk2(z256), pk2(cmul(z256, z2)), pk2(cmul(z256, z4)), pk2(cmul(z256, z6)));
#pragma unroll
  for (int s = 0; s < 4; s++) d0 = mfma(fa[s], fx[s], d0);
  f2 z768 = cmul(z512, z256);
  wr4<4>(scr, pk2(cmul(z256, z8)), pk2(cmul(z256, z10)), pk2(cmul(z256, z12)), pk2(cmul(z256, z14)));
  wr4<8>(scr, pk2(z512), pk2(cmul(z512, z4)), pk2(cmul(z512, z8)), pk2(cmul(z512, z12)));
  wr4<12>(scr, pk2(z768), pk2(cmul(z768, z4)), pk2(cmul(z768, z8)), pk2(cmul(z768, z12)));
  read4(ra, fa);
  f2 z1024 = cmul(z512, z512), z1536 = cmul(z768, z768);
  f2 z1280 = cmul(z1024, z256), z1792 = cmul(z1024, z768), z2048 = cmul(z1024, z1024);
  wr4<0>(scr, pk2(z1024), pk2(cmul(z1024, z8)), pk2(z1280), pk2(cmul(z1280, z8)));
#pragma unroll
  for (int s = 0; s < 4; s++) d1 = mfma(fa[s], fx[s], d1);
  unsigned t2048 = pk2(z2048);
  wr4<4>(scr, pk2(z1536), pk2(cmul(z1536, z8)), pk2(z1792), pk2(cmul(z1792, z8)));
  asm volatile("s_mov_b32 m0, %0\n\ts_nop 0\n\tds_write_addtid_b32 %1 offset:%2" :: "s"(scr), "v"(t2048), "i"(8 * 288) : "memory");
  read4(ra, fa);
#pragma unroll
  for (int s = 0; s < 4; s++) d2 = mfma(fa[s], fx[s], d2);
  const int pa = part + pslot(lane >> 4, lane & 15);
  *LDSP(f4, pa) = d0;
  *LDSP(f4, pa + 1024) = d1;
  *LDSP(f4, pa + 2048) = d2;
}

DI f4 sum3_4(int a) { return *LDSP(f4, a) + *LDSP(f4, a + 3072) + *LDSP(f4, a + 6144); }
DI f2 sum3_2(int a) { return *LDSP(f2, a) + *LDSP(f2, a + 3072) + *LDSP(f2, a + 6144); }
DI float sum3_1(int a) { return *LDSP(float, a) + *LDSP(float, a + 3072) + *LDSP(float, a + 6144); }

DI void put_unit(int kn, int i, float v0, float v1, float v2, float v3, float v4, float v5, float v6) {
  unsigned p10 = pk2(v1, v0), p21 = pk2(v2, v1), p32 = pk2(v3, v2), p43 = pk2(v4, v3), p54 = pk2(v5, v4), p65 = pk2(v6, v5);
  int a = kn + (135 - i) * 8;
  *LDSP(u2, a) = u2{p32, p10};
  *LDSP(u2, a + KNC) = u2{p43, p21};
  *LDSP(u2, a + 2 * KNC) = u2{p54, p32};
  *LDSP(u2, a + 3 * KNC) = u2{p65, p43};
}

DI u4 band(int lane, int lo, int n) {
  int b = lane & 15, g = lane >> 4;
  unsigned m = (((1u << n) - 1u) << ((lo + b) & 31));
  m >>= 8 * g;
  u4 r;
#pragma unroll
  for (int j = 0; j < 4; j++) r[j] = ((m >> (2 * j)) & 1u) * 0x3f80u + ((m >> (2 * j + 1)) & 1u) * 0x3f800000u;
  return r;
}

DI void store_rows(const Params& p, int nch, int ch0, int t, const float (&dk)[6]) {
  const int ya = OFF_YS + ((t >> 9) & 1) * 6144 + (t & 511) * 2;
  const int ua = OFF_UT + (UT_PAD + t) * 2;
  float o[6];
#pragma unroll
  for (int c = 0; c < 6; c++) {
    if (c < 4 || nch == 6) {
      float y = bf2f(*LDSP(unsigned short, ya + c * 1024));
      float uu = bf2f(*LDSP(unsigned short, ua + c * UTS));
      o[c] = y + rbf(uu * dk[c]);
    } else o[c] = 0.f;
  }
  char* dst = (char*)p.out + (size_t)t * 2560 + ch0 * 2;
  if (nch == 6) *(u3*)dst = u3{pk2(o[0], o[1]), pk2(o[2], o[3]), pk2(o[4], o[5])};
  else *(u2*)dst = u2{pk2(o[0], o[1]), pk2(o[2], o[3])};
}

__global__ __launch_bounds__(768, 3) void ssm_fused(Params p) {
  extern __shared__ __attribute__((aligned(16))) char smem[];
  const int tid = threadIdx.x, lane = tid & 63;
  const int w = __builtin_amdgcn_readfirstlane(tid >> 6);
  const int bx = blockIdx.x & 7, bj = blockIdx.x >> 3;
  const int blk = bx * 5 + bj / 6, sl = bj % 6;
  const int nch = sl < 4 ? 6 : 4;
  const int ch0 = blk * 32 + (sl < 4 ? 6 * sl : 24 + 4 * (sl - 4));
  const int br = blk >= 20;
  const int e0 = ch0 - br * 640;
  const char* ub = (const char*)p.u[br];
  STAMP(0);

  const int ntask = nch * 3;
  const bool two = (w + 12 < ntask);
  Raw r0 = load_raw(p, br, e0 + w / 3, w % 3, lane);
  Raw r1 = r0;
  if (two) r1 = load_raw(p, br, e0 + (w + 12) / 3, (w + 12) % 3, lane);
  unsigned pfv = 0;
  {
    int i = tid;
    int row = bj * 69 + i / 5;
    if (i < 69 * 5 && row < 2048) pfv = *(const unsigned*)(ub + (size_t)row * 1280 + (bx & 3) * 320 + (i % 5) * 64);
  }
  if (tid < 6 * 36) *LDSP(u4, OFF_UT + (tid / 36) * UTS + (tid % 36) * 16) = u4{0, 0, 0, 0};
  float dk[6];
#pragma unroll
  for (int c = 0; c < 6; c++) dk[c] = bf2f(p.dsk[br][e0 + (c < nch ? c : 0)]);
  u4 bw2[4], bw3[2];
  bw2[0] = band(lane, 16 - 4 - 1, 3);
  bw2[1] = band(lane, 16 - 8 - 2, 5);
  bw2[2] = band(lane, 16 - 12 - 1, 3);
  bw2[3] = band(lane, 24 - 16 - 2, 5);
  bw3[0] = band(lane, 16 - 8 - 3, 7);
  bw3[1] = band(lane, 24 - 16 - 4, 9);
  asm volatile("s_waitcnt vmcnt(0)" :: "v"(pfv) : "memory");
  STAMP(1);
  u3 ur[4];
  if (w >= 4) {
    const char* src = ub + (size_t)(tid - 256) * 4 * 1280 + e0 * 2;
#pragma unroll
    for (int r = 0; r < 4; r++) {
      if (nch == 6) ur[r] = *(const u3*)(src + r * 1280);
      else { u2 t = *(const u2*)(src + r * 1280); ur[r] = u3{t.x, t.y, 0u}; }
    }
  }
  const int scr = OFF_RX + w * SCR;
  kgen_task(r0, br, scr, OFF_RY + w * 3072, lane);
  if (two) kgen_task(r1, br, scr, OFF_RY + (w + 12) * 3072, lane);
  STAMP(2);
  if (w >= 4) {
    const int ua = OFF_UT + (UT_PAD + (tid - 256) * 4) * 2;
#pragma unroll
    for (int d = 0; d < 3; d++) {
      *LDSP(u2, ua + (2 * d) * UTS) = u2{__builtin_amdgcn_perm(ur[1][d], ur[0][d], 0x05040100u), __builtin_amdgcn_perm(ur[3][d], ur[2][d], 0x05040100u)};
      *LDSP(u2, ua + (2 * d + 1) * UTS) = u2{__builtin_amdgcn_perm(ur[1][d], ur[0][d], 0x07060302u), __builtin_amdgcn_perm(ur[3][d], ur[2][d], 0x07060302u)};
    }
  }
  bar();
  STAMP(3);

  const int c = w < 6 ? w : w - 6;
  const int la = lane & 15, lg = lane >> 4;
  const int part = OFF_RY + c * 3 * 3072;
  const int ubase = OFF_UT + c * UTS + UT_PAD * 2 + la * 32 + lg * 16;
  const f4 zero4 = {0.f, 0.f, 0.f, 0.f};
  if (w < 6) {
    u4 ka[17], kbr;
    if (c < nch) {
      const int kn = OFF_KN + c * 4 * KNC;
      f4 va = sum3_4(part + pslot(lane & 3, lane >> 2));
      f2 vb = sum3_2(part + 1024 + pslot((lane >> 1) & 1, lane >> 2) + (lane & 1) * 8);
      float k512 = sum3_1(part + 1024 + pslot(2, 0));
      float k1024 = sum3_1(part + 2048 + pslot(0, 0));
      float e0v = __builtin_amdgcn_readfirstlane(__float_as_int(vb.x)) * 0 + 0.f;
      (void)e0v;
      float k256 = __int_as_float(__builtin_amdgcn_readfirstlane(__float_as_int(vb.x)));
      float k258 = __int_as_float(__builtin_amdgcn_readfirstlane(__float_as_int(vb.y)));
      float k0 = __int_as_float(__builtin_amdgcn_readfirstlane(__float_as_int(va.x)));
      float k1 = __int_as_float(__builtin_amdgcn_readfirstlane(__float_as_int(va.y)));
      float k2 = __int_as_float(__builtin_amdgcn_readfirstlane(__float_as_int(va.z)));
      float n0 = nextlane(va.x), n1 = nextlane(va.y), n2 = nextlane(va.z);
      float ne = nextlane(vb.x), no = nextlane(vb.y);
      if (lane == 63) { n0 = k256; n1 = k256; n2 = k258; ne = k512; no = k512; }
      put_unit(kn, lane, va.x, va.y, va.z, va.w, n0, n1, n2);
      put_unit(kn, 64 + lane, vb.x, vb.x, vb.y, ne, ne, ne, no);
      if (lane < 13) {
        int i = lane < 5 ? lane - 5 : 123 + lane;
        float a = (i == 128) ? k512 : 0.f;
        bool m1 = (i == -1);
        put_unit(kn, i, a, a, a, 0.f, m1 ? k0 : 0.f, m1 ? k1 : 0.f, m1 ? k2 : 0.f);
      }
      const int s = (la + 1) & 3;
      const int kr = kn + s * KNC + (543 - (la - 8 * lg + 16) + s) * 2;
#pragma unroll
      for (int m = 0; m < 17; m++) {
        u2 lo = *LDSP(u2, kr - 64 * m), hi = *LDSP(u2, kr - 64 * m + 8);
        ka[m] = u4{lo.x, lo.y, hi.x, hi.y};
      }
      unsigned kb = pk2(k1024, k1024);
      u4 msk = band(lane, 4, 7);
#pragma unroll
      for (int j = 0; j < 4; j++) kbr[j] = ((msk[j] & 0x80u) ? (kb & 0xffffu) : 0u) | ((msk[j] & 0x800000u) ? (kb & 0xffff0000u) : 0u);
    }
    bar();
    STAMP(4);
#pragma unroll
    for (int st = 0; st < 4; st++) {
      if (c < nch) {
        f4 a0 = zero4, a1 = zero4;
        if (st > 0) {
          const int fp = OFF_RY + (st & 1) * 12288 + c * 2048 + lane * 16;
          a0 = *LDSP(f4, fp); a1 = *LDSP(f4, fp + 1024);
        }
#pragma unroll
        for (int n = 0; n < 25; n++) {
          if (st == 0 && n > 16) continue;
          u4 v = *LDSP(u4, ubase + 32 * (32 * st + 15 - 2 * n));
          if (n < 17) a1 = mfma(ka[n], v, a1);
          if (n >= 8) a0 = mfma(ka[n - 8], v, a0);
        }
        if (st >= 2) a0 = mfma(kbr, *LDSP(u4, ubase + 2 * (512 * st - 1032)), a0);
        if (st >= 1) a1 = mfma(kbr, *LDSP(u4, ubase + 2 * (512 * st + 256 - 1032)), a1);
        const int ya = OFF_YS + (st & 1) * 6144 + c * 1024 + la * 32 + lg * 8;
        *LDSP(u2, ya) = u2{pk2(a0[0], a0[1]), pk2(a0[2], a0[3])};
        *LDSP(u2, ya + 512) = u2{pk2(a1[0], a1[1]), pk2(a1[2], a1[3])};
      }
      bar();
      STAMP(5 + st);
    }
  } else {
    u4 t2[3][2], t3[4];
    if (c < nch) {
      const int kf = OFF_KF + c * KFS;
      f2 v3 = sum3_2(part + 2048 + pslot(lane >> 5, lane & 15) + ((lane >> 4) & 1) * 8);
      f4 v2 = sum3_4(part + 1024 + pslot(2 + ((lane >> 4) & 1), lane & 15));
      float k2048 = sum3_1(part + 2048 + pslot(2, 0));
      float n3 = nextlane(v3.x), n2 = nextlane(v2.x);
      if (lane == 63) n3 = k2048;
      if (lane == 31) n2 = 0.f;
      *LDSP(unsigned, kf + 512 + (63 - lane) * 4) = pk2(v3.y, n3);
      if (lane < 16) *LDSP(unsigned, kf + 512 + (64 + lane) * 4) = 0u;
      {
        int x = lane < 32 ? 47 - lane : (lane < 48 ? lane - 32 : lane);
        bool nz = lane < 32;
        *LDSP(unsigned, kf + x * 4) = nz ? pk2(v2.y, v2.z) : 0u;
        *LDSP(unsigned, kf + 256 + x * 4) = nz ? pk2(v2.w, n2) : 0u;
      }
      const int dd = la - 4 * lg;
#pragma unroll
      for (int d = 0; d < 3; d++)
#pragma unroll
        for (int h = 0; h < 2; h++) {
          const int a = kf + h * 256 + (79 - 16 * (d + 2) - dd) * 4;
#pragma unroll
          for (int j = 0; j < 4; j++) t2[d][h][j] = *LDSP(unsigned, a + 4 * j);
        }
#pragma unroll
      for (int d = 0; d < 4; d++) {
        const int a = kf + 512 + (127 - 16 * (d + 4) - dd) * 4;
#pragma unroll
        for (int j = 0; j < 4; j++) t3[d][j] = *LDSP(unsigned, a + 4 * j);
      }
    }
    bar();
    u4 w2[6][2], w3[4];
#pragma unroll
    for (int st = 0; st < 4; st++) {
      if (st > 0) {
        for (int rw = c; rw < 8; rw += 6) store_rows(p, nch, ch0, 512 * (st - 1) + 64 * rw + lane, dk);
      }
      if (st < 3 && c < nch) {
#pragma unroll
        for (int be = 2 * st; be < 2 * st + 2; be++) {
          u4 ua = *LDSP(u4, ubase + 2 * (256 * be - 16));
          u4 uc = *LDSP(u4, ubase + 2 * (256 * be - 24));
          f4 q0 = mfma(ua, bw2[0], zero4), q1 = mfma(ua, bw2[1], zero4), q2 = mfma(ua, bw2[2], zero4), q3 = mfma(uc, bw2[3], zero4);
#pragma unroll
          for (int j = 0; j < 4; j++) { w2[be][0][j] = pk2(q0[j], q1[j]); w2[be][1][j] = pk2(q2[j], q3[j]); }
          if (be < 4) {
            f4 q4 = mfma(ua, bw3[0], zero4), q5 = mfma(uc, bw3[1], zero4);
#pragma unroll
            for (int j = 0; j < 4; j++) w3[be][j] = pk2(q4[j], q5[j]);
          }
        }
        const int fp = OFF_RY + ((st + 1) & 1) * 12288 + c * 2048 + lane * 16;
#pragma unroll
        for (int h = 0; h < 2; h++) {
          const int q = 2 * (st + 1) + h;
          f4 acc = zero4;
#pragma unroll
          for (int d = 2; d <= 4; d++)
            if (q - d >= 0) { acc = mfma(w2[q - d][0], t2[d - 2][0], acc); acc = mfma(w2[q - d][1], t2[d - 2][1], acc); }
#pragma unroll
          for (int d = 4; d <= 7; d++)
            if (q - d >= 0) acc = mfma(w3[q - d], t3[d - 4], acc);
          *LDSP(f4, fp + h * 1024) = acc;
        }
      }
      bar();
    }
  }
  if (w < 8) store_rows(p, nch, ch0, 1536 + 64 * w + lane, dk);
  STAMP(9);
}

extern "C" void kernel_launch(void* const* d_in, const int* in_sizes, int n_in,
                              void* d_out, int out_size, void* d_ws, size_t ws_size,
                              hipStream_t stream) {
  const unsigned short* const* in = (const unsigned short* const*)d_in;
  Params p;
  p.u[0] = in[0];  p.a_re[0] = in[1]; p.a_im[0] = in[2]; p.c_re[0] = in[3];  p.c_im[0] = in[4];  p.ldt[0] = in[5];  p.dsk[0] = in[6];
  p.u[1] = in[7];  p.a_re[1] = in[8]; p.a_im[1] = in[9]; p.c_re[1] = in[12]; p.c_im[1] = in[13]; p.ldt[1] = in[14]; p.dsk[1] = in[15];
  p.b_re = in[10]; p.b_im = in[11];
  p.out = (unsigned short*)d_out;
  p.dbg = (unsigned long long*)d_ws;
  (void)hipFuncSetAttribute((const void*)ssm_fused, hipFuncAttributeMaxDynamicSharedMemorySize, LDS_BYTES);
  ssm_fused<<<240, 768, LDS_BYTES, stream>>>(p);
}
```

```cpp
#include <hip/hip_runtime.h>
#include <hip/hip_bf16.h>
#include <stdint.h>

typedef float f2 __attribute__((ext_vector_type(2)));
typedef float f4 __attribute__((ext_vector_type(4)));
typedef __bf16 bf2 __attribute__((ext_vector_type(2)));
typedef __bf16 bf8 __attribute__((ext_vector_type(8)));
typedef unsigned u2 __attribute__((ext_vector_type(2)));
typedef unsigned u3 __attribute__((ext_vector_type(3)));
typedef unsigned u4 __attribute__((ext_vector_type(4)));
typedef unsigned short u16;
#define DI __device__ __forceinline__
#define LDSP(T, off) ((__attribute__((address_space(3))) T*)(unsigned long)(unsigned)(off))
#define CSTP(T, ptr) ((const __attribute__((address_space(4))) T*)(unsigned long)(ptr))
#define SB() __builtin_amdgcn_sched_barrier(0)

#define STAMP(i)
#define STAMP_DECL
#define STAMP_DUMP
#define CODE_PF_WAVES 0

constexpr int NMODE = 192;
constexpr int SCR = 16 * 288;
constexpr int KNC = 1216;
constexpr int OFF_KN = 0;
constexpr int OFF_KF = 6 * 4 * KNC;
constexpr int KFS = 896;
constexpr int OFF_YS = OFF_KF + 6 * KFS;
constexpr int OFF_RY = 12 * SCR;
constexpr int UT_PAD = 528;
constexpr int UTS = (UT_PAD + 2048) * 2;
constexpr int OFF_UT = OFF_RY + 18 * 3072;
constexpr int LDS_BYTES = OFF_UT + 6 * UTS;
static_assert(OFF_YS + 12288 <= OFF_RY, "lds alias");

#define CM_MUL(d, a, b) "v_pk_mul_f32 " d ", " a ", " b " op_sel_hi:[0,1]\n\t"
#define CM_FMA(d, a, b) "v_pk_fma_f32 " d ", " a ", " b ", " d " op_sel:[1,1,0] op_sel_hi:[1,0,1] neg_lo:[0,1,0]\n\t"
#define CC_MUL(d, a, b) "v_pk_mul_f32 " d ", " a ", " b " op_sel_hi:[0,1] neg_hi:[0,1]\n\t"
#define CC_FMA(d, a, b) "v_pk_fma_f32 " d ", " a ", " b ", " d " op_sel:[1,1,0] op_sel_hi:[1,0,1]\n\t"
DI f2 cmul(f2 a, f2 b) {
  f2 r;
  asm(CM_MUL("%0", "%1", "%2") CM_FMA("%0", "%1", "%2") : "=&v"(r) : "v"(a), "v"(b));
  return r;
}
DI void cmul2(f2& r0, f2& r1, f2 a0, f2 b0, f2 a1, f2 b1) {
  asm(CM_MUL("%0", "%2", "%3") CM_MUL("%1", "%4", "%5") CM_FMA("%0", "%2", "%3") CM_FMA("%1", "%4", "%5")
      : "=&v"(r0), "=&v"(r1) : "v"(a0), "v"(b0), "v"(a1), "v"(b1));
}
DI void cmul3(f2& r0, f2& r1, f2& r2, f2 a, f2 b0, f2 b1, f2 b2) {
  asm(CM_MUL("%0", "%3", "%4") CM_MUL("%1", "%3", "%5") CM_MUL("%2", "%3", "%6")
      CM_FMA("%0", "%3", "%4") CM_FMA("%1", "%3", "%5") CM_FMA("%2", "%3", "%6")
      : "=&v"(r0), "=&v"(r1), "=&v"(r2) : "v"(a), "v"(b0), "v"(b1), "v"(b2));
}
DI void cmul4(f2& r0, f2& r1, f2& r2, f2& r3, f2 a, f2 b0, f2 b1, f2 b2, f2 b3) {
  asm(CM_MUL("%0", "%4", "%5") CM_MUL("%1", "%4", "%6") CM_MUL("%2", "%4", "%7") CM_MUL("%3", "%4", "%8")
      CM_FMA("%0", "%4", "%5") CM_FMA("%1", "%4", "%6") CM_FMA("%2", "%4", "%7") CM_FMA("%3", "%4", "%8")
      : "=&v"(r0), "=&v"(r1), "=&v"(r2), "=&v"(r3) : "v"(a), "v"(b0), "v"(b1), "v"(b2), "v"(b3));
}
DI void cmulc4(f2& r0, f2& r1, f2& r2, f2& r3, f2 a0, f2 a1, f2 a2, f2 a3, f2 b) {
  asm(CC_MUL("%0", "%4", "%8") CC_MUL("%1", "%5", "%8") CC_MUL("%2", "%6", "%8") CC_MUL("%3", "%7", "%8")
      CC_FMA("%0", "%4", "%8") CC_FMA("%1", "%5", "%8") CC_FMA("%2", "%6", "%8") CC_FMA("%3", "%7", "%8")
      : "=&v"(r0), "=&v"(r1), "=&v"(r2), "=&v"(r3) : "v"(a0), "v"(a1), "v"(a2), "v"(a3), "v"(b));
}
DI void cmulc_sq(f2& r0, f2& r1, f2 a, f2 b) {
  asm(CC_MUL("%0", "%2", "%3") CM_MUL("%1", "%3", "%3") CC_FMA("%0", "%2", "%3") CM_FMA("%1", "%3", "%3")
      : "=&v"(r0), "=&v"(r1) : "v"(a), "v"(b));
}
DI void cmulc2_sq(f2& r0, f2& r1, f2& r2, f2 a0, f2 a1, f2 b) {
  asm(CC_MUL("%0", "%3", "%5") CC_MUL("%1", "%4", "%5") CM_MUL("%2", "%5", "%5")
      CC_FMA("%0", "%3", "%5") CC_FMA("%1", "%4", "%5") CM_FMA("%2", "%5", "%5")
      : "=&v"(r0), "=&v"(r1), "=&v"(r2) : "v"(a0), "v"(a1), "v"(b));
}
DI unsigned pk2(float lo, float hi) {
  bf2 r = __builtin_convertvector((f2{lo, hi}), bf2);
  return __builtin_bit_cast(unsigned, r);
}
DI unsigned pk2(f2 a) { return pk2(a.x, a.y); }
DI float bfhi(unsigned x) { return __uint_as_float(x & 0xffff0000u); }
DI float bflo(unsigned x) { return __uint_as_float(x << 16); }
DI float rbf(float f) { return __uint_as_float(pk2(f, 0.f) << 16); }
DI f4 mfma(u4 a, u4 b, f4 c) {
  return __builtin_amdgcn_mfma_f32_16x16x32_bf16(__builtin_bit_cast(bf8, a), __builtin_bit_cast(bf8, b), c, 0, 0, 0);
}
DI void bar() { asm volatile("s_waitcnt lgkmcnt(0)\n\ts_barrier" ::: "memory"); }
template <int R0>
DI void wr4(int m0v, unsigned a, unsigned b, unsigned c, unsigned d) {
  asm volatile("s_mov_b32 m0, %0\n\ts_nop 0\n\t"
               "ds_write_addtid_b32 %1 offset:%5\n\tds_write_addtid_b32 %2 offset:%6\n\t"
               "ds_write_addtid_b32 %3 offset:%7\n\tds_write_addtid_b32 %4 offset:%8"
               :: "s"(m0v), "v"(a), "v"(b), "v"(c), "v"(d),
                  "i"(R0 * 288), "i"((R0 + 1) * 288), "i"((R0 + 2) * 288), "i"((R0 + 3) * 288) : "memory");
}
DI f2 cexp_dir(float a2, float b2) {
  float m = __builtin_amdgcn_exp2f(a2);
  float ph = __builtin_amdgcn_fractf(b2);
  return f2{m * __builtin_amdgcn_cosf(ph), m * __builtin_amdgcn_sinf(ph)};
}
DI float exp_acc(float x) {
  float h = x * 1.44269504088896341f;
  float l = fmaf(x, 1.92596299112661746e-8f, fmaf(x, 1.44269504088896341f, -h));
  float e = __builtin_amdgcn_exp2f(h);
  return fmaf(e * l, 0.69314718055994531f, e);
}
DI float nextlane(float v) {
  return __int_as_float(__builtin_amdgcn_update_dpp(0, __float_as_int(v), 0x130, 0xf, 0xf, true));
}
DI float first(float v) { return __int_as_float(__builtin_amdgcn_readfirstlane(__float_as_int(v))); }
DI int pslot(int g, int a) { return 16 * (16 * g + ((a + 4 * g) & 15)); }

struct Br { const char *u, *are, *aim, *cre, *cim, *bre, *bim, *ldt, *dsk; };
struct Params { Br b[2]; char* out; unsigned long long* dbg; };
struct Raw { unsigned ar, ai, cr, ci, br, bi; };

DI void read4(int ra, u4 (&f)[4]) {
#pragma unroll
  for (int s = 0; s < 4; s++) f[s] = *LDSP(u4, ra + 64 * s);
}

DI void kgen_task(const Raw& r, float ldt, int br, int scr, int ra, int pa) {
  float dt = rbf(exp_acc(ldt));
  float ar, cx, cy;
  if (br == 0) {
    ar = -rbf(exp_acc(bflo(r.ar)));
    float d2 = dt + dt;
    cx = bflo(r.cr) * d2; cy = bflo(r.ci) * d2;
  } else {
    ar = bflo(r.ar);
    float bx = bflo(r.br) * dt, by = bflo(r.bi) * dt, c0 = bflo(r.cr), c1 = bflo(r.ci);
    cx = c0 * bx - c1 * by; cy = c0 * by + c1 * bx;
  }
  float a2 = (ar * dt) * 1.44269504088896341f;
  float b2 = (bflo(r.ai) * dt) * 0.15915494309189535f;
  u4 fa[4], fx[4];
  const f4 zero4 = {0.f, 0.f, 0.f, 0.f};
  f4 d0 = zero4, d1 = zero4, d2 = zero4;
  f2 z1 = cexp_dir(a2, b2);
  f2 z256 = cexp_dir(a2 * 256.f, b2 * 256.f);
  f2 z2, z3, z4, z5, z6, z7, z8, z9, z10, z11, z12, z13, z14, z15, w, w2, w4, z512, z768, z1024, z1280, z1536, z1792, z2048;
  cmul2(z2, z512, z1, z1, z256, z256);
  cmul2(z3, z4, z2, z1, z2, z2);
  wr4<0>(scr, 0x00003f80u, pk2(z1), pk2(z2), pk2(z3));
  cmul4(z8, z5, z6, z7, z4, z4, z1, z2, z3);
  wr4<4>(scr, pk2(z4), pk2(z5), pk2(z6), pk2(z7));
  cmul4(w, z9, z10, z11, z8, z8, z1, z2, z3);
  wr4<8>(scr, pk2(z8), pk2(z9), pk2(z10), pk2(z11));
  f2 x0 = f2{cx, -cy}, x1, x2, x3;
  cmulc_sq(x1, w2, x0, w);
  cmul4(z12, z13, z14, z15, z8, z4, z5, z6, z7);
  wr4<12>(scr, pk2(z12), pk2(z13), pk2(z14), pk2(z15));
  read4(ra, fa);
  cmulc2_sq(x2, x3, w4, x0, x1, w2);
  wr4<0>(scr, pk2(x0), pk2(x1), pk2(x2), pk2(x3));
  f2 y0, y1, y2, y3;
  cmulc4(y0, y1, y2, y3, x0, x1, x2, x3, w4);
  wr4<4>(scr, pk2(y0), pk2(y1), pk2(y2), pk2(y3));
  cmulc4(x0, x1, x2, x3, y0, y1, y2, y3, w4);
  wr4<8>(scr, pk2(x0), pk2(x1), pk2(x2), pk2(x3));
  cmulc4(y0, y1, y2, y3, x0, x1, x2, x3, w4);
  wr4<12>(scr, pk2(y0), pk2(y1), pk2(y2), pk2(y3));
  read4(ra, fx);
  cmul4(z768, y1, y2, y3, z256, z512, z2, z4, z6);
  wr4<0>(scr, pk2(z256), pk2(y1), pk2(y2), pk2(y3));
  cmul4(y0, y1, y2, y3, z256, z8, z10, z12, z14);
  wr4<4>(scr, pk2(y0), pk2(y1), pk2(y2), pk2(y3));
#pragma unroll
  for (int s = 0; s < 4; s++) d0 = mfma(fa[s], fx[s], d0);
  cmul4(z1024, y1, y2, y3, z512, z512, z4, z8, z12);
  wr4<8>(scr, pk2(z512), pk2(y1), pk2(y2), pk2(y3));
  cmul4(z1536, y1, y2, y3, z768, z768, z4, z8, z12);
  wr4<12>(scr, pk2(z768), pk2(y1), pk2(y2), pk2(y3));
  read4(ra, fa);
  cmul4(z1280, z1792, z2048, y0, z1024, z256, z768, z1024, z8);
  cmul3(y1, y2, y3, z8, z1280, z1536, z1792);
  unsigned t0 = pk2(z1024), t1 = pk2(y0), t2 = pk2(z1280), t3 = pk2(y1), t4 = pk2(z1536), t5 = pk2(y2), t6 = pk2(z1792), t7 = pk2(y3), t8 = pk2(z2048);
#pragma unroll
  for (int s = 0; s < 4; s++) d1 = mfma(fa[s], fx[s], d1);
  wr4<0>(scr, t0, t1, t2, t3);
  wr4<4>(scr, t4, t5, t6, t7);
  asm volatile("s_mov_b32 m0, %0\n\ts_nop 0\n\tds_write_addtid_b32 %1 offset:%2" :: "s"(scr), "v"(t8), "i"(8 * 288) : "memory");
  read4(ra, fa);
  *LDSP(f4, pa) = d0;
  *LDSP(f4, pa + 1024) = d1;
#pragma unroll
  for (int s = 0; s < 4; s++) d2 = mfma(fa[s], fx[s], d2);
  *LDSP(f4, pa + 2048) = d2;
}

DI f4 sum3_4(int a) { return *LDSP(f4, a) + *LDSP(f4, a + 3072) + *LDSP(f4, a + 6144); }
DI f2 sum3_2(int a) { return *LDSP(f2, a) + *LDSP(f2, a + 3072) + *LDSP(f2, a + 6144); }
DI float sum3_1(int a) { return *LDSP(float, a) + *LDSP(float, a + 3072) + *LDSP(float, a + 6144); }

DI void put_unit(int a, float v0, float v1, float v2, float v3, float v4, float v5, float v6) {
  unsigned p10 = pk2(v1, v0), p21 = pk2(v2, v1), p32 = pk2(v3, v2), p43 = pk2(v4, v3), p54 = pk2(v5, v4), p65 = pk2(v6, v5);
  *LDSP(u2, a) = u2{p32, p10};
  *LDSP(u2, a + KNC) = u2{p43, p21};
  *LDSP(u2, a + 2 * KNC) = u2{p54, p32};
  *LDSP(u2, a + 3 * KNC) = u2{p65, p43};
}

DI u4 band(int lane, int lo, int n) {
  unsigned m = (((1u << n) - 1u) << lo << (lane & 15)) >> (8 * (lane >> 4));
  u4 r;
#pragma unroll
  for (int j = 0; j < 4; j++)
    r[j] = ((unsigned)__builtin_amdgcn_sbfe(m, 2 * j, 1) & 0xffffu) | ((unsigned)__builtin_amdgcn_sbfe(m, 2 * j + 1, 1) << 16);
  return r;
}
DI u4 band1(int lane, int lo, int n) { u4 r = band(lane, lo, n) & 0x3f803f80u; asm volatile("" : "+v"(r)); return r; }

DI void store_rows(char* outp, bool six, int t, unsigned dk01, unsigned dk23, unsigned dk45) {
  const int ya = OFF_YS + ((t >> 9) & 1) * 6144 + (t & 511) * 2;
  const int ua = OFF_UT + (UT_PAD + t) * 2;
  unsigned y[6], u[6];
#pragma unroll
  for (int c = 0; c < 6; c++) { y[c] = *LDSP(u16, ya + c * 1024); u[c] = *LDSP(u16, ua + c * UTS); }
  unsigned s01 = pk2(bflo(u[0]) * bflo(dk01), bflo(u[1]) * bfhi(dk01));
  unsigned s23 = pk2(bflo(u[2]) * bflo(dk23), bflo(u[3]) * bfhi(dk23));
  unsigned s45 = pk2(bflo(u[4]) * bflo(dk45), bflo(u[5]) * bfhi(dk45));
  unsigned o01 = pk2(bflo(y[0]) + bflo(s01), bflo(y[1]) + bfhi(s01));
  unsigned o23 = pk2(bflo(y[2]) + bflo(s23), bflo(y[3]) + bfhi(s23));
  unsigned o45 = pk2(bflo(y[4]) + bflo(s45), bflo(y[5]) + bfhi(s45));
  char* dst = outp + (unsigned)t * 2560u;
  if (six) *(u3*)dst = u3{o01, o23, o45};
  else *(u2*)dst = u2{o01, o23};
}

template <bool FIRST>
DI void near_stage(const u4 (&ka)[17], const u4& kbr, u4 (&ring)[9], int ub, int fp, int ya) {
  f4 a0 = {0.f, 0.f, 0.f, 0.f}, a1 = a0;
  if (!FIRST) { a0 = *LDSP(f4, fp); a1 = *LDSP(f4, fp + 1024); }
  SB();
#pragma unroll
  for (int n = 0; n < 27; n++) {
    const u4 v = ring[n % 9];
    if (n < 17) a1 = mfma(ka[n], v, a1);
    if (n >= 8 && n < (FIRST ? 17 : 25)) a0 = mfma(ka[n - 8], v, a0);
    if (!FIRST && n == 25) a0 = mfma(kbr, v, a0);
    if (!FIRST && n == 26) a1 = mfma(kbr, v, a1);
    const int m = n + 9;
    if (m < 25) { if (!FIRST || m < 17) ring[n % 9] = *LDSP(u4, ub + 1600 - 64 * m); }
    else if (m < 27) { if (!FIRST) ring[n % 9] = *LDSP(u4, ub + 1600 - 480 - 2064 + 512 * (m - 25)); }
    else ring[n % 9] = *LDSP(u4, ub + 1600 + 1024 - 64 * (m - 27));
    SB();
  }
  *LDSP(u2, ya) = u2{pk2(a0[0], a0[1]), pk2(a0[2], a0[3])};
  *LDSP(u2, ya + 512) = u2{pk2(a1[0], a1[1]), pk2(a1[2], a1[3])};
}

__global__ __launch_bounds__(768, 3) void ssm_fused(Params p) {
  __shared__ __attribute__((aligned(16))) char smem[LDS_BYTES];
  asm volatile("" :: "v"((__attribute__((address_space(3))) char*)smem));
  const int tid = threadIdx.x, lane = tid & 63;
  const int w = __builtin_amdgcn_readfirstlane(tid >> 6);
  const int bx = blockIdx.x & 7, bj = blockIdx.x >> 3;
  const int blk = bx * 5 + bj / 6, sl = bj % 6;
  const bool six = sl < 4;
  const int nch = six ? 6 : 4;
  const int ch0 = blk * 32 + (six ? 6 * sl : 8 + 4 * sl);
  const int br = blk >= 20;
  const int e0 = ch0 - br * 640;
  const Br B = p.b[br];
  STAMP_DECL
  STAMP(0);

  const bool two = six && w < 6;
  const int tk1 = two ? w + 12 : w;
  const unsigned o0 = (e0 * NMODE + w * 64 + lane) * 2u, o1 = (e0 * NMODE + tk1 * 64 + lane) * 2u;
  Raw r0, r1;
  r0.ar = *(const u16*)(B.are + o0); r0.ai = *(const u16*)(B.aim + o0); r0.cr = *(const u16*)(B.cre + o0);
  r0.ci = *(const u16*)(B.cim + o0); r0.br = *(const u16*)(B.bre + o0); r0.bi = *(const u16*)(B.bim + o0);
  unsigned pf0, pf1 = 0, pf2 = 0;
  {
    unsigned row = bj * 69 + tid / 5;
    row = row < 2047u ? row : 2047u;
    unsigned off = row * 1280u + (bx & 3) * 320u + (tid % 5) * 64u;
    if (tid >= 345) off = e0 * 2u;
    asm volatile("global_load_dword %0, %1, %2" : "=v"(pf0) : "v"(off), "s"(B.u) : "memory");
    if (w >= 12 - CODE_PF_WAVES) {
      unsigned long long pc;
      asm volatile("s_getpc_b64 %0" : "=s"(pc));
      unsigned co = ((11 - w) * 64 + lane) * 64u;
      asm volatile("global_load_dword %0, %1, %2" : "=v"(pf1) : "v"(co), "s"(pc & ~63ull) : "memory");
    }
    if (w == 6) {
      unsigned oo = lane * 81920u + ch0 * 2u;
      asm volatile("global_load_dword %0, %1, %2" : "=v"(pf2) : "v"(oo), "s"(p.out) : "memory");
    }
  }
  r1.ar = *(const u16*)(B.are + o1); r1.ai = *(const u16*)(B.aim + o1); r1.cr = *(const u16*)(B.cre + o1);
  r1.ci = *(const u16*)(B.cim + o1); r1.br = *(const u16*)(B.bre + o1); r1.bi = *(const u16*)(B.bim + o1);
  const int c0 = w / 3, c1 = tk1 / 3;
  const unsigned l0 = CSTP(unsigned, B.ldt)[(e0 + c0) >> 1], l1 = CSTP(unsigned, B.ldt)[(e0 + c1) >> 1];
  const unsigned dk01 = CSTP(unsigned, B.dsk)[e0 >> 1], dk23 = CSTP(unsigned, B.dsk)[(e0 >> 1) + 1],
                 dk45 = CSTP(unsigned, B.dsk)[(e0 >> 1) + (six ? 2 : 1)];
  SB();
  if (tid < 6 * 66) *LDSP(u4, OFF_UT + (tid / 66) * UTS + (tid % 66) * 16) = u4{0, 0, 0, 0};
  u4 cst[6];
  if (w >= 6) {
    cst[0] = band1(lane, 16 - 4 - 1, 3);
    cst[1] = band1(lane, 16 - 8 - 2, 5);
    cst[2] = band1(lane, 16 - 12 - 1, 3);
    cst[3] = band1(lane, 24 - 16 - 2, 5);
    cst[4] = band1(lane, 16 - 8 - 3, 7);
    cst[5] = band1(lane, 24 - 16 - 4, 9);
  } else {
    cst[0] = band(lane, 4, 7);
    asm volatile("" : "+v"(cst[0]));
  }
  asm volatile("s_waitcnt vmcnt(0)" : "+v"(pf0), "+v"(pf1), "+v"(pf2) :: "memory");
  STAMP(1);
  u3 ur[4];
  if (w >= 4) {
    const unsigned so = (tid - 256) * 5120u + e0 * 2u - (six ? 0u : 4u);
#pragma unroll
    for (int r = 0; r < 4; r++) ur[r] = *(const u3*)(B.u + (so + r * 1280u));
  }
  {
    const int scr = w * SCR;
    const int ra = scr + (lane & 15) * 288 + (lane >> 4) * 16;
    int pa = OFF_RY + w * 3072 + pslot(lane >> 4, lane & 15);
    float ldt = (c0 & 1) ? bfhi(l0) : bflo(l0);
    for (int it = 0;; it++) {
      kgen_task(r0, ldt, br, scr, ra, pa);
      if (it == 1 || !two) break;
      r0 = r1; ldt = (c1 & 1) ? bfhi(l1) : bflo(l1); pa += 12 * 3072;
    }
  }
  STAMP(2);
  if (w >= 4) {
    const int ua = OFF_UT + (UT_PAD + (tid - 256) * 4) * 2 - (six ? 0 : 2 * UTS);
#pragma unroll
    for (int d = 0; d < 3; d++) {
      if (d > 0 || six) {
        *LDSP(u2, ua + (2 * d) * UTS) = u2{__builtin_amdgcn_perm(ur[1][d], ur[0][d], 0x05040100u), __builtin_amdgcn_perm(ur[3][d], ur[2][d], 0x05040100u)};
        *LDSP(u2, ua + (2 * d + 1) * UTS) = u2{__builtin_amdgcn_perm(ur[1][d], ur[0][d], 0x07060302u), __builtin_amdgcn_perm(ur[3][d], ur[2][d], 0x07060302u)};
      }
    }
  }
  const int c = w < 6 ? w : w - 6;
  const int la = lane & 15, lg = lane >> 4;
  const int part = OFF_RY + c * 3 * 3072;
  const int ubase = OFF_UT + c * UTS + UT_PAD * 2 + la * 32 + lg * 16;
  char* outp = p.out + ch0 * 2;
  const f4 zero4 = {0.f, 0.f, 0.f, 0.f};
  bar();
  STAMP(3);

  if (w < 6) {
    if (c < nch) {
      u4 ka[17], kbr, ring[9];
      const int kn = OFF_KN + c * 4 * KNC;
      f4 va = sum3_4(part + pslot(lane & 3, lane >> 2));
      f2 vb = sum3_2(part + 1024 + pslot((lane >> 1) & 1, lane >> 2) + (lane & 1) * 8);
      float k512 = sum3_1(part + 1024 + pslot(2, 0));
      float k1024 = sum3_1(part + 2048 + pslot(0, 0));
      int ub = ubase + 480 - 1600;
#pragma unroll
      for (int n = 0; n < 9; n++) ring[n] = *LDSP(u4, ub + 1600 - 64 * n);
      float k256 = first(vb.x), k258 = first(vb.y), k0 = first(va.x), k1 = first(va.y), k2 = first(va.z);
      float n0 = nextlane(va.x), n1 = nextlane(va.y), n2 = nextlane(va.z);
      float ne = nextlane(vb.x), no = nextlane(vb.y);
      if (lane == 63) { n0 = k256; n1 = k256; n2 = k258; ne = k512; no = k512; }
      STAMP(10);
      put_unit(kn + (135 - lane) * 8, va.x, va.y, va.z, va.w, n0, n1, n2);
      put_unit(kn + (71 - lane) * 8, vb.x, vb.x, vb.y, ne, ne, ne, no);
      if (lane < 13) {
        int i = lane < 5 ? lane - 5 : 123 + lane;
        float a = (i == 128) ? k512 : 0.f;
        bool m1 = (i == -1);
        put_unit(kn + (135 - i) * 8, a, a, a, 0.f, m1 ? k0 : 0.f, m1 ? k1 : 0.f, m1 ? k2 : 0.f);
      }
      STAMP(11);
      const int s = (la + 1) & 3;
      const int kr = kn + s * KNC + (543 - (la - 8 * lg + 16) + s) * 2 - 1024;
#pragma unroll
      for (int m = 0; m < 17; m++) {
        u2 lo = *LDSP(u2, kr + 1024 - 64 * m), hi = *LDSP(u2, kr + 1024 - 64 * m + 8);
        ka[m] = u4{lo.x, lo.y, hi.x, hi.y};
      }
      kbr = cst[0] & pk2(k1024, k1024);
      int fp = OFF_RY + c * 2048 + lane * 16;
      int ya = OFF_YS + c * 1024 + la * 32 + lg * 8;
      STAMP(12);
      bar();
      STAMP(4);
      near_stage<true>(ka, kbr, ring, ub, fp, ya);
      STAMP(13);
      bar();
      STAMP(5);
      for (int st = 1; st < 4; st++) {
        ub += 1024;
        near_stage<false>(ka, kbr, ring, ub, OFF_RY + (st & 1) * 12288 + c * 2048 + lane * 16, OFF_YS + (st & 1) * 6144 + c * 1024 + la * 32 + lg * 8);
        if (st == 1) STAMP(14);
        bar();
        if (st == 1) STAMP(6); if (st == 2) STAMP(7); if (st == 3) STAMP(8);
      }
    } else {
      for (int i = 0; i < 5; i++) bar();
    }
  } else {
    u4 t2[3][2], t3[4];
    const bool act = c < nch;
    u4 ua0, uc0, ua1, uc1;
    if (act) {
      const int kf = OFF_KF + c * KFS;
      f2 v3 = sum3_2(part + 2048 + pslot(lane >> 5, lane & 15) + ((lane >> 4) & 1) * 8);
      f4 v2 = sum3_4(part + 1024 + pslot(2 + ((lane >> 4) & 1), lane & 15));
      float k2048 = sum3_1(part + 2048 + pslot(2, 0));
      ua0 = *LDSP(u4, ubase - 32); uc0 = *LDSP(u4, ubase - 48); ua1 = *LDSP(u4, ubase + 512 - 32); uc1 = *LDSP(u4, ubase + 512 - 48);
      float n3 = nextlane(v3.x), n2 = nextlane(v2.x);
      if (lane == 63) n3 = k2048;
      if (lane == 31) n2 = 0.f;
      *LDSP(unsigned, kf + 512 + (63 - lane) * 4) = pk2(v3.y, n3);
      if (lane < 16) *LDSP(unsigned, kf + 512 + (64 + lane) * 4) = 0u;
      {
        int x = lane < 32 ? 47 - lane : (lane < 48 ? lane - 32 : lane);
        bool nz = lane < 32;
        *LDSP(unsigned, kf + x * 4) = nz ? pk2(v2.y, v2.z) : 0u;
        *LDSP(unsigned, kf + 256 + x * 4) = nz ? pk2(v2.w, n2) : 0u;
      }
      int a = kf + (15 - la + 4 * lg) * 4;
      asm volatile("" : "+v"(a));
#pragma unroll
      for (int d = 0; d < 3; d++)
#pragma unroll
        for (int h = 0; h < 2; h++)
#pragma unroll
          for (int j = 0; j < 4; j++) t2[d][h][j] = *LDSP(unsigned, a + h * 256 + (32 - 16 * d + j) * 4);
#pragma unroll
      for (int d = 0; d < 4; d++)
#pragma unroll
        for (int j = 0; j < 4; j++) t3[d][j] = *LDSP(unsigned, a + 512 + (48 - 16 * d + j) * 4);
    }
    STAMP(12);
    bar();
    STAMP(4);
    u4 w2[6][2], w3[4];
#pragma unroll
    for (int st = 0; st < 4; st++) {
      if (st > 0) {
        for (int rw = c; rw < 8; rw += 6) store_rows(outp, six, 512 * (st - 1) + 64 * rw + lane, dk01, dk23, dk45);
      }
      STAMP(13 + 2 * st);
      if (st < 3 && act) {
#pragma unroll
        for (int h = 0; h < 2; h++) {
          const int be = 2 * st + h;
          const u4 ua = h ? ua1 : ua0, uc = h ? uc1 : uc0;
          f4 q0 = mfma(ua, cst[0], zero4), q1 = mfma(ua, cst[1], zero4), q2 = mfma(ua, cst[2], zero4), q3 = mfma(uc, cst[3], zero4);
          f4 q4, q5;
          if (be < 4) { q4 = mfma(ua, cst[4], zero4); q5 = mfma(uc, cst[5], zero4); }
#pragma unroll
          for (int j = 0; j < 4; j++) { w2[be][0][j] = pk2(q0[j], q1[j]); w2[be][1][j] = pk2(q2[j], q3[j]); }
          if (be < 4) {
#pragma unroll
            for (int j = 0; j < 4; j++) w3[be][j] = pk2(q4[j], q5[j]);
          }
        }
        if (st < 2) {
          const int nb = ubase + 1024 * (st + 1);
          ua0 = *LDSP(u4, nb - 32); uc0 = *LDSP(u4, nb - 48); ua1 = *LDSP(u4, nb + 512 - 32); uc1 = *LDSP(u4, nb + 512 - 48);
        }
        const int fp = OFF_RY + ((st + 1) & 1) * 12288 + c * 2048 + lane * 16;
        f4 acc[2] = {zero4, zero4};
#pragma unroll
        for (int d = 7; d >= 4; d--)
#pragma unroll
          for (int h = 0; h < 2; h++) { const int q = 2 * (st + 1) + h; if (q - d >= 0) acc[h] = mfma(w3[q - d], t3[d - 4], acc[h]); }
#pragma unroll
        for (int d = 4; d >= 2; d--)
#pragma unroll
          for (int e = 0; e < 2; e++)
#pragma unroll
            for (int h = 0; h < 2; h++) { const int q = 2 * (st + 1) + h; if (q - d >= 0) acc[h] = mfma(w2[q - d][e], t2[d - 2][e], acc[h]); }
        *LDSP(f4, fp) = acc[0];
        *LDSP(f4, fp + 1024) = acc[1];
      }
      STAMP(14 + 2 * st);
      bar();
      STAMP(5 + st);
    }
  }
  if (w < 8) store_rows(outp, six, 1536 + 64 * w + lane, dk01, dk23, dk45);
  STAMP(9);
  STAMP_DUMP;
}

extern "C" void kernel_launch(void* const* d_in, const int* in_sizes, int n_in,
                              void* d_out, int out_size, void* d_ws, size_t ws_size,
                              hipStream_t stream) {
  const char* const* in = (const char* const*)d_in;
  Params p;
  p.b[0] = Br{in[0], in[1], in[2], in[3], in[4], in[3], in[4], in[5], in[6]};
  p.b[1] = Br{in[7], in[8], in[9], in[12], in[13], in[10], in[11], in[14], in[15]};
  p.out = (char*)d_out;
  p.dbg = (unsigned long long*)d_ws;
  ssm_fused<<<240, 768, 0, stream>>>(p);
}
```

```cpp
#define PROBE 99
#include <hip/hip_runtime.h>
#include <hip/hip_bf16.h>
#include <stdint.h>

typedef float f2 __attribute__((ext_vector_type(2)));
typedef float f4 __attribute__((ext_vector_type(4)));
typedef __bf16 bf2 __attribute__((ext_vector_type(2)));
typedef __bf16 bf8 __attribute__((ext_vector_type(8)));
typedef unsigned u2 __attribute__((ext_vector_type(2)));
typedef unsigned u3 __attribute__((ext_vector_type(3)));
typedef unsigned u4 __attribute__((ext_vector_type(4)));
typedef unsigned short u16;
#define DI __device__ __forceinline__
#define LDSP(T, off) ((__attribute__((address_space(3))) T*)(unsigned long)(unsigned)(off))
#define CSTP(T, ptr) ((const __attribute__((address_space(4))) T*)(unsigned long)(ptr))
#define SB() __builtin_amdgcn_sched_barrier(0)

#define STAMP_DECL unsigned long long rt0_ = __builtin_amdgcn_s_memrealtime(), rtk_ = rt0_;
#define STAMP(i) do { if ((i) == PROBE) { __builtin_amdgcn_sched_barrier(0); asm volatile("s_memrealtime %0\n\ts_waitcnt lgkmcnt(0)" : "=s"(rtk_) :: "memory"); __builtin_amdgcn_sched_barrier(0);} } while (0)
#define STAMP_DUMP do { asm volatile("s_waitcnt vmcnt(0)" ::: "memory"); bar(); STAMP(30); if (w == 0) { unsigned long long tgt = rt0_ + 3000ull + 10ull * (rtk_ - rt0_); \
   for (int it_ = 0; it_ < 200000; it_++) { if (__builtin_amdgcn_s_memrealtime() >= tgt) break; __builtin_amdgcn_s_sleep(1); } } } while (0)

#define CODE_PF_WAVES 3

constexpr int NMODE = 192;
constexpr int SCR = 16 * 288;
constexpr int KNC = 1216;
constexpr int OFF_KN = 0;
constexpr int OFF_KF = 6 * 4 * KNC;
constexpr int KFS = 896;
constexpr int OFF_YS = OFF_KF + 6 * KFS;
constexpr int OFF_RY = 12 * SCR;
constexpr int UT_PAD = 528;
constexpr int UTS = (UT_PAD + 2048) * 2;
constexpr int OFF_UT = OFF_RY + 18 * 3072;
constexpr int LDS_BYTES = OFF_UT + 6 * UTS;
static_assert(OFF_YS + 12288 <= OFF_RY, "lds alias");

#define CM_MUL(d, a, b) "v_pk_mul_f32 " d ", " a ", " b " op_sel_hi:[0,1]\n\t"
#define CM_FMA(d, a, b) "v_pk_fma_f32 " d ", " a ", " b ", " d " op_sel:[1,1,0] op_sel_hi:[1,0,1] neg_lo:[0,1,0]\n\t"
#define CC_MUL(d, a, b) "v_pk_mul_f32 " d ", " a ", " b " op_sel_hi:[0,1] neg_hi:[0,1]\n\t"
#define CC_FMA(d, a, b) "v_pk_fma_f32 " d ", " a ", " b ", " d " op_sel:[1,1,0] op_sel_hi:[1,0,1]\n\t"
DI f2 cmul(f2 a, f2 b) {
  f2 r;
  asm(CM_MUL("%0", "%1", "%2") CM_FMA("%0", "%1", "%2") : "=&v"(r) : "v"(a), "v"(b));
  return r;
}
DI void cmul2(f2& r0, f2& r1, f2 a0, f2 b0, f2 a1, f2 b1) {
  asm(CM_MUL("%0", "%2", "%3") CM_MUL("%1", "%4", "%5") CM_FMA("%0", "%2", "%3") CM_FMA("%1", "%4", "%5")
      : "=&v"(r0), "=&v"(r1) : "v"(a0), "v"(b0), "v"(a1), "v"(b1));
}
DI void cmul3(f2& r0, f2& r1, f2& r2, f2 a, f2 b0, f2 b1, f2 b2) {
  asm(CM_MUL("%0", "%3", "%4") CM_MUL("%1", "%3", "%5") CM_MUL("%2", "%3", "%6")
      CM_FMA("%0", "%3", "%4") CM_FMA("%1", "%3", "%5") CM_FMA("%2", "%3", "%6")
      : "=&v"(r0), "=&v"(r1), "=&v"(r2) : "v"(a), "v"(b0), "v"(b1), "v"(b2));
}
DI void cmul4(f2& r0, f2& r1, f2& r2, f2& r3, f2 a, f2 b0, f2 b1, f2 b2, f2 b3) {
  asm(CM_MUL("%0", "%4", "%5") CM_MUL("%1", "%4", "%6") CM_MUL("%2", "%4", "%7") CM_MUL("%3", "%4", "%8")
      CM_FMA("%0", "%4", "%5") CM_FMA("%1", "%4", "%6") CM_FMA("%2", "%4", "%7") CM_FMA("%3", "%4", "%8")
      : "=&v"(r0), "=&v"(r1), "=&v"(r2), "=&v"(r3) : "v"(a), "v"(b0), "v"(b1), "v"(b2), "v"(b3));
}
DI void cmulc4(f2& r0, f2& r1, f2& r2, f2& r3, f2 a0, f2 a1, f2 a2, f2 a3, f2 b) {
  asm(CC_MUL("%0", "%4", "%8") CC_MUL("%1", "%5", "%8") CC_MUL("%2", "%6", "%8") CC_MUL("%3", "%7", "%8")
      CC_FMA("%0", "%4", "%8") CC_FMA("%1", "%5", "%8") CC_FMA("%2", "%6", "%8") CC_FMA("%3", "%7", "%8")
      : "=&v"(r0), "=&v"(r1), "=&v"(r2), "=&v"(r3) : "v"(a0), "v"(a1), "v"(a2), "v"(a3), "v"(b));
}
DI void cmulc_sq(f2& r0, f2& r1, f2 a, f2 b) {
  asm(CC_MUL("%0", "%2", "%3") CM_MUL("%1", "%3", "%3") CC_FMA("%0", "%2", "%3") CM_FMA("%1", "%3", "%3")
      : "=&v"(r0), "=&v"(r1) : "v"(a), "v"(b));
}
DI void cmulc2_sq(f2& r0, f2& r1, f2& r2, f2 a0, f2 a1, f2 b) {
  asm(CC_MUL("%0", "%3", "%5") CC_MUL("%1", "%4", "%5") CM_MUL("%2", "%5", "%5")
      CC_FMA("%0", "%3", "%5") CC_FMA("%1", "%4", "%5") CM_FMA("%2", "%5", "%5")
      : "=&v"(r0), "=&v"(r1), "=&v"(r2) : "v"(a0), "v"(a1), "v"(b));
}
DI unsigned pk2(float lo, float hi) {
  bf2 r = __builtin_convertvector((f2{lo, hi}), bf2);
  return __builtin_bit_cast(unsigned, r);
}
DI unsigned pk2(f2 a) { return pk2(a.x, a.y); }
DI float bfhi(unsigned x) { return __uint_as_float(x & 0xffff0000u); }
DI float bflo(unsigned x) { return __uint_as_float(x << 16); }
DI float rbf(float f) { return __uint_as_float(pk2(f, 0.f) << 16); }
DI f4 mfma(u4 a, u4 b, f4 c) {
  return __builtin_amdgcn_mfma_f32_16x16x32_bf16(__builtin_bit_cast(bf8, a), __builtin_bit_cast(bf8, b), c, 0, 0, 0);
}
DI void bar() { asm volatile("s_waitcnt lgkmcnt(0)\n\ts_barrier" ::: "memory"); }
template <int R0>
DI void wr4(int m0v, unsigned a, unsigned b, unsigned c, unsigned d) {
  asm volatile("s_mov_b32 m0, %0\n\ts_nop 0\n\t"
               "ds_write_addtid_b32 %1 offset:%5\n\tds_write_addtid_b32 %2 offset:%6\n\t"
               "ds_write_addtid_b32 %3 offset:%7\n\tds_write_addtid_b32 %4 offset:%8"
               :: "s"(m0v), "v"(a), "v"(b), "v"(c), "v"(d),
                  "i"(R0 * 288), "i"((R0 + 1) * 288), "i"((R0 + 2) * 288), "i"((R0 + 3) * 288) : "memory");
}
DI f2 cexp_dir(float a2, float b2) {
  float m = __builtin_amdgcn_exp2f(a2);
  float ph = __builtin_amdgcn_fractf(b2);
  return f2{m * __builtin_amdgcn_cosf(ph), m * __builtin_amdgcn_sinf(ph)};
}
DI float exp_acc(float x) {
  float h = x * 1.44269504088896341f;
  float l = fmaf(x, 1.92596299112661746e-8f, fmaf(x, 1.44269504088896341f, -h));
  float e = __builtin_amdgcn_exp2f(h);
  return fmaf(e * l, 0.69314718055994531f, e);
}
DI float nextlane(float v) {
  return __int_as_float(__builtin_amdgcn_update_dpp(0, __float_as_int(v), 0x130, 0xf, 0xf, true));
}
DI float first(float v) { return __int_as_float(__builtin_amdgcn_readfirstlane(__float_as_int(v))); }
DI int pslot(int g, int a) { return 16 * (16 * g + ((a + 4 * g) & 15)); }

struct Br { const char *u, *are, *aim, *cre, *cim, *bre, *bim, *ldt, *dsk; };
struct Params { Br b[2]; char* out; unsigned long long* dbg; };
struct Raw { unsigned ar, ai, cr, ci, br, bi; };

DI void read4(int ra, u4 (&f)[4]) {
#pragma unroll
  for (int s = 0; s < 4; s++) f[s] = *LDSP(u4, ra + 64 * s);
}

DI void kgen_task(const Raw& r, float ldt, int br, int scr, int ra, int pa) {
  float dt = rbf(exp_acc(ldt));
  float ar, cx, cy;
  if (br == 0) {
    ar = -rbf(exp_acc(bflo(r.ar)));
    float d2 = dt + dt;
    cx = bflo(r.cr) * d2; cy = bflo(r.ci) * d2;
  } else {
    ar = bflo(r.ar);
    float bx = bflo(r.br) * dt, by = bflo(r.bi) * dt, c0 = bflo(r.cr), c1 = bflo(r.ci);
    cx = c0 * bx - c1 * by; cy = c0 * by + c1 * bx;
  }
  float a2 = (ar * dt) * 1.44269504088896341f;
  float b2 = (bflo(r.ai) * dt) * 0.15915494309189535f;
  u4 fa[4], fx[4];
  const f4 zero4 = {0.f, 0.f, 0.f, 0.f};
  f4 d0 = zero4, d1 = zero4, d2 = zero4;
  f2 z1 = cexp_dir(a2, b2);
  f2 z256 = cexp_dir(a2 * 256.f, b2 * 256.f);
  f2 z2, z3, z4, z5, z6, z7, z8, z9, z10, z11, z12, z13, z14, z15, w, w2, w4, z512, z768, z1024, z1280, z1536, z1792, z2048;
  cmul2(z2, z512, z1, z1, z256, z256);
  cmul2(z3, z4, z2, z1, z2, z2);
  wr4<0>(scr, 0x00003f80u, pk2(z1), pk2(z2), pk2(z3));
  cmul4(z8, z5, z6, z7, z4, z4, z1, z2, z3);
  wr4<4>(scr, pk2(z4), pk2(z5), pk2(z6), pk2(z7));
  cmul4(w, z9, z10, z11, z8, z8, z1, z2, z3);
  wr4<8>(scr, pk2(z8), pk2(z9), pk2(z10), pk2(z11));
  f2 x0 = f2{cx, -cy}, x1, x2, x3;
  cmulc_sq(x1, w2, x0, w);
  cmul4(z12, z13, z14, z15, z8, z4, z5, z6, z7);
  wr4<12>(scr, pk2(z12), pk2(z13), pk2(z14), pk2(z15));
  read4(ra, fa);
  cmulc2_sq(x2, x3, w4, x0, x1, w2);
  wr4<0>(scr, pk2(x0), pk2(x1), pk2(x2), pk2(x3));
  f2 y0, y1, y2, y3;
  cmulc4(y0, y1, y2, y3, x0, x1, x2, x3, w4);
  wr4<4>(scr, pk2(y0), pk2(y1), pk2(y2), pk2(y3));
  cmulc4(x0, x1, x2, x3, y0, y1, y2, y3, w4);
  wr4<8>(scr, pk2(x0), pk2(x1), pk2(x2), pk2(x3));
  cmulc4(y0, y1, y2, y3, x0, x1, x2, x3, w4);
  wr4<12>(scr, pk2(y0), pk2(y1), pk2(y2), pk2(y3));
  read4(ra, fx);
  cmul4(z768, y1, y2, y3, z256, z512, z2, z4, z6);
  wr4<0>(scr, pk2(z256), pk2(y1), pk2(y2), pk2(y3));
  cmul4(y0, y1, y2, y3, z256, z8, z10, z12, z14);
  wr4<4>(scr, pk2(y0), pk2(y1), pk2(y2), pk2(y3));
#pragma unroll
  for (int s = 0; s < 4; s++) d0 = mfma(fa[s], fx[s], d0);
  cmul4(z1024, y1, y2, y3, z512, z512, z4, z8, z12);
  wr4<8>(scr, pk2(z512), pk2(y1), pk2(y2), pk2(y3));
  cmul4(z1536, y1, y2, y3, z768, z768, z4, z8, z12);
  wr4<12>(scr, pk2(z768), pk2(y1), pk2(y2), pk2(y3));
  read4(ra, fa);
  cmul4(z1280, z1792, z2048, y0, z1024, z256, z768, z1024, z8);
  cmul3(y1, y2, y3, z8, z1280, z1536, z1792);
  unsigned t0 = pk2(z1024), t1 = pk2(y0), t2 = pk2(z1280), t3 = pk2(y1), t4 = pk2(z1536), t5 = pk2(y2), t6 = pk2(z1792), t7 = pk2(y3), t8 = pk2(z2048);
#pragma unroll
  for (int s = 0; s < 4; s++) d1 = mfma(fa[s], fx[s], d1);
  wr4<0>(scr, t0, t1, t2, t3);
  wr4<4>(scr, t4, t5, t6, t7);
  asm volatile("s_mov_b32 m0, %0\n\ts_nop 0\n\tds_write_addtid_b32 %1 offset:%2" :: "s"(scr), "v"(t8), "i"(8 * 288) : "memory");
  read4(ra, fa);
  *LDSP(f4, pa) = d0;
  *LDSP(f4, pa + 1024) = d1;
#pragma unroll
  for (int s = 0; s < 4; s++) d2 = mfma(fa[s], fx[s], d2);
  *LDSP(f4, pa + 2048) = d2;
}

DI f4 sum3_4(int a) { return *LDSP(f4, a) + *LDSP(f4, a + 3072) + *LDSP(f4, a + 6144); }
DI f2 sum3_2(int a) { return *LDSP(f2, a) + *LDSP(f2, a + 3072) + *LDSP(f2, a + 6144); }
DI float sum3_1(int a) { return *LDSP(float, a) + *LDSP(float, a + 3072) + *LDSP(float, a + 6144); }

DI void put_unit(int a, float v0, float v1, float v2, float v3, float v4, float v5, float v6) {
  unsigned p10 = pk2(v1, v0), p21 = pk2(v2, v1), p32 = pk2(v3, v2), p43 = pk2(v4, v3), p54 = pk2(v5, v4), p65 = pk2(v6, v5);
  *LDSP(u2, a) = u2{p32, p10};
  *LDSP(u2, a + KNC) = u2{p43, p21};
  *LDSP(u2, a + 2 * KNC) = u2{p54, p32};
  *LDSP(u2, a + 3 * KNC) = u2{p65, p43};
}

DI u4 band(int lane, int lo, int n) {
  unsigned m = (((1u << n) - 1u) << lo << (lane & 15)) >> (8 * (lane >> 4));
  u4 r;
#pragma unroll
  for (int j = 0; j < 4; j++)
    r[j] = ((unsigned)__builtin_amdgcn_sbfe(m, 2 * j, 1) & 0xffffu) | ((unsigned)__builtin_amdgcn_sbfe(m, 2 * j + 1, 1) << 16);
  return r;
}
DI u4 band1(int lane, int lo, int n) { u4 r = band(lane, lo, n) & 0x3f803f80u; asm volatile("" : "+v"(r)); return r; }

DI void store_rows(char* outp, bool six, int t, unsigned dk01, unsigned dk23, unsigned dk45) {
  const int ya = OFF_YS + ((t >> 9) & 1) * 6144 + (t & 511) * 2;
  const int ua = OFF_UT + (UT_PAD + t) * 2;
  unsigned y[6], u[6];
#pragma unroll
  for (int c = 0; c < 6; c++) { y[c] = *LDSP(u16, ya + c * 1024); u[c] = *LDSP(u16, ua + c * UTS); }
  unsigned s01 = pk2(bflo(u[0]) * bflo(dk01), bflo(u[1]) * bfhi(dk01));
  unsigned s23 = pk2(bflo(u[2]) * bflo(dk23), bflo(u[3]) * bfhi(dk23));
  unsigned s45 = pk2(bflo(u[4]) * bflo(dk45), bflo(u[5]) * bfhi(dk45));
  unsigned o01 = pk2(bflo(y[0]) + bflo(s01), bflo(y[1]) + bfhi(s01));
  unsigned o23 = pk2(bflo(y[2]) + bflo(s23), bflo(y[3]) + bfhi(s23));
  unsigned o45 = pk2(bflo(y[4]) + bflo(s45), bflo(y[5]) + bfhi(s45));
  char* dst = outp + (unsigned)t * 2560u;
  if (six) *(u3*)dst = u3{o01, o23, o45};
  else *(u2*)dst = u2{o01, o23};
}

template <bool FIRST>
DI void near_stage(const u4 (&ka)[17], const u4& kbr, u4 (&ring)[9], int ub, int fp, int ya) {
  f4 a0 = {0.f, 0.f, 0.f, 0.f}, a1 = a0;
  if (!FIRST) { a0 = *LDSP(f4, fp); a1 = *LDSP(f4, fp + 1024); }
  SB();
#pragma unroll
  for (int n = 0; n < 27; n++) {
    const u4 v = ring[n % 9];
    if (n < 17) a1 = mfma(ka[n], v, a1);
    if (n >= 8 && n < (FIRST ? 17 : 25)) a0 = mfma(ka[n - 8], v, a0);
    if (!FIRST && n == 25) a0 = mfma(kbr, v, a0);
    if (!FIRST && n == 26) a1 = mfma(kbr, v, a1);
    const int m = n + 9;
    if (m < 25) { if (!FIRST || m < 17) ring[n % 9] = *LDSP(u4, ub + 1600 - 64 * m); }
    else if (m < 27) { if (!FIRST) ring[n % 9] = *LDSP(u4, ub + 1600 - 480 - 2064 + 512 * (m - 25)); }
    else ring[n % 9] = *LDSP(u4, ub + 1600 + 1024 - 64 * (m - 27));
    SB();
  }
  *LDSP(u2, ya) = u2{pk2(a0[0], a0[1]), pk2(a0[2], a0[3])};
  *LDSP(u2, ya + 512) = u2{pk2(a1[0], a1[1]), pk2(a1[2], a1[3])};
}

__global__ __launch_bounds__(768, 3) void ssm_fused(Params p) {
  __shared__ __attribute__((aligned(16))) char smem[LDS_BYTES];
  asm volatile("" :: "v"((__attribute__((address_space(3))) char*)smem));
  const int tid = threadIdx.x, lane = tid & 63;
  const int w = __builtin_amdgcn_readfirstlane(tid >> 6);
  const int bx = blockIdx.x & 7, bj = blockIdx.x >> 3;
  const int blk = bx * 5 + bj / 6, sl = bj % 6;
  const bool six = sl < 4;
  const int nch = six ? 6 : 4;
  const int ch0 = blk * 32 + (six ? 6 * sl : 8 + 4 * sl);
  const int br = blk >= 20;
  const int e0 = ch0 - br * 640;
  const Br B = p.b[br];
  STAMP_DECL
  STAMP(0);

  asm volatile("" :: "s"(B.are), "s"(B.dsk));
  STAMP(29);
  const bool two = six && w < 6;
  const int tk1 = two ? w + 12 : w;
  const unsigned o0 = (e0 * NMODE + w * 64 + lane) * 2u, o1 = (e0 * NMODE + tk1 * 64 + lane) * 2u;
  Raw r0, r1;
  r0.ar = *(const u16*)(B.are + o0); r0.ai = *(const u16*)(B.aim + o0); r0.cr = *(const u16*)(B.cre + o0);
  r0.ci = *(const u16*)(B.cim + o0); r0.br = *(const u16*)(B.bre + o0); r0.bi = *(const u16*)(B.bim + o0);
  unsigned pf0, pf1 = 0, pf2 = 0;
  {
    unsigned row = bj * 69 + tid / 5;
    row = row < 2047u ? row : 2047u;
    unsigned off = row * 1280u + (bx & 3) * 320u + (tid % 5) * 64u;
    if (tid >= 345) off = e0 * 2u;
    asm volatile("global_load_dword %0, %1, %2" : "=v"(pf0) : "v"(off), "s"(B.u) : "memory");
    if (w >= 12 - CODE_PF_WAVES) {
      unsigned long long pc;
      asm volatile("s_getpc_b64 %0" : "=s"(pc));
      unsigned co = ((11 - w) * 64 + lane) * 64u;
      asm volatile("global_load_dword %0, %1, %2" : "=v"(pf1) : "v"(co), "s"(pc & ~63ull) : "memory");
    }
    if (w == 6) {
      unsigned oo = lane * 81920u + ch0 * 2u;
      asm volatile("global_load_dword %0, %1, %2" : "=v"(pf2) : "v"(oo), "s"(p.out) : "memory");
    }
  }
  r1.ar = *(const u16*)(B.are + o1); r1.ai = *(const u16*)(B.aim + o1); r1.cr = *(const u16*)(B.cre + o1);
  r1.ci = *(const u16*)(B.cim + o1); r1.br = *(const u16*)(B.bre + o1); r1.bi = *(const u16*)(B.bim + o1);
  const int c0 = w / 3, c1 = tk1 / 3;
  const unsigned l0 = CSTP(unsigned, B.ldt)[(e0 + c0) >> 1], l1 = CSTP(unsigned, B.ldt)[(e0 + c1) >> 1];
  const unsigned dk01 = CSTP(unsigned, B.dsk)[e0 >> 1], dk23 = CSTP(unsigned, B.dsk)[(e0 >> 1) + 1],
                 dk45 = CSTP(unsigned, B.dsk)[(e0 >> 1) + (six ? 2 : 1)];
  SB();
  if (tid < 6 * 66) *LDSP(u4, OFF_UT + (tid / 66) * UTS + (tid % 66) * 16) = u4{0, 0, 0, 0};
  u4 cst[6];
  if (w >= 6) {
    cst[0] = band1(lane, 16 - 4 - 1, 3);
    cst[1] = band1(lane, 16 - 8 - 2, 5);
    cst[2] = band1(lane, 16 - 12 - 1, 3);
    cst[3] = band1(lane, 24 - 16 - 2, 5);
    cst[4] = band1(lane, 16 - 8 - 3, 7);
    cst[5] = band1(lane, 24 - 16 - 4, 9);
  } else {
    cst[0] = band(lane, 4, 7);
    asm volatile("" : "+v"(cst[0]));
  }
  asm volatile("s_waitcnt vmcnt(0)" : "+v"(pf0), "+v"(pf1), "+v"(pf2) :: "memory");
  STAMP(1);
  u3 ur[4];
  if (w >= 4) {
    const unsigned so = (tid - 256) * 5120u + e0 * 2u - (six ? 0u : 4u);
#pragma unroll
    for (int r = 0; r < 4; r++) ur[r] = *(const u3*)(B.u + (so + r * 1280u));
  }
  {
    const int scr = w * SCR;
    const int ra = scr + (lane & 15) * 288 + (lane >> 4) * 16;
    int pa = OFF_RY + w * 3072 + pslot(lane >> 4, lane & 15);
    float ldt = (c0 & 1) ? bfhi(l0) : bflo(l0);
    for (int it = 0;; it++) {
      kgen_task(r0, ldt, br, scr, ra, pa);
      if (it == 1 || !two) break;
      r0 = r1; ldt = (c1 & 1) ? bfhi(l1) : bflo(l1); pa += 12 * 3072;
    }
  }
  STAMP(2);
  if (w >= 4) {
    const int ua = OFF_UT + (UT_PAD + (tid - 256) * 4) * 2 - (six ? 0 : 2 * UTS);
#pragma unroll
    for (int d = 0; d < 3; d++) {
      if (d > 0 || six) {
        *LDSP(u2, ua + (2 * d) * UTS) = u2{__builtin_amdgcn_perm(ur[1][d], ur[0][d], 0x05040100u), __builtin_amdgcn_perm(ur[3][d], ur[2][d], 0x05040100u)};
        *LDSP(u2, ua + (2 * d + 1) * UTS) = u2{__builtin_amdgcn_perm(ur[1][d], ur[0][d], 0x07060302u), __builtin_amdgcn_perm(ur[3][d], ur[2][d], 0x07060302u)};
      }
    }
  }
  const int c = w < 6 ? w : w - 6;
  const int la = lane & 15, lg = lane >> 4;
  const int part = OFF_RY + c * 3 * 3072;
  const int ubase = OFF_UT + c * UTS + UT_PAD * 2 + la * 32 + lg * 16;
  char* outp = p.out + ch0 * 2;
  const f4 zero4 = {0.f, 0.f, 0.f, 0.f};
  bar();
  STAMP(3);

  if (w < 6) {
    if (c < nch) {
      u4 ka[17], kbr, ring[9];
      const int kn = OFF_KN + c * 4 * KNC;
      f4 va = sum3_4(part + pslot(lane & 3, lane >> 2));
      f2 vb = sum3_2(part + 1024 + pslot((lane >> 1) & 1, lane >> 2) + (lane & 1) * 8);
      float k512 = sum3_1(part + 1024 + pslot(2, 0));
      float k1024 = sum3_1(part + 2048 + pslot(0, 0));
      int ub = ubase + 480 - 1600;
#pragma unroll
      for (int n = 0; n < 9; n++) ring[n] = *LDSP(u4, ub + 1600 - 64 * n);
      float k256 = first(vb.x), k258 = first(vb.y), k0 = first(va.x), k1 = first(va.y), k2 = first(va.z);
      float n0 = nextlane(va.x), n1 = nextlane(va.y), n2 = nextlane(va.z);
      float ne = nextlane(vb.x), no = nextlane(vb.y);
      if (lane == 63) { n0 = k256; n1 = k256; n2 = k258; ne = k512; no = k512; }
      STAMP(10);
      put_unit(kn + (135 - lane) * 8, va.x, va.y, va.z, va.w, n0, n1, n2);
      put_unit(kn + (71 - lane) * 8, vb.x, vb.x, vb.y, ne, ne, ne, no);
      if (lane < 13) {
        int i = lane < 5 ? lane - 5 : 123 + lane;
        float a = (i == 128) ? k512 : 0.f;
        bool m1 = (i == -1);
        put_unit(kn + (135 - i) * 8, a, a, a, 0.f, m1 ? k0 : 0.f, m1 ? k1 : 0.f, m1 ? k2 : 0.f);
      }
      STAMP(11);
      const int s = (la + 1) & 3;
      const int kr = kn + s * KNC + (543 - (la - 8 * lg + 16) + s) * 2 - 1024;
#pragma unroll
      for (int m = 0; m < 17; m++) {
        u2 lo = *LDSP(u2, kr + 1024 - 64 * m), hi = *LDSP(u2, kr + 1024 - 64 * m + 8);
        ka[m] = u4{lo.x, lo.y, hi.x, hi.y};
      }
      kbr = cst[0] & pk2(k1024, k1024);
      int fp = OFF_RY + c * 2048 + lane * 16;
      int ya = OFF_YS + c * 1024 + la * 32 + lg * 8;
      STAMP(12);
      bar();
      STAMP(4);
      near_stage<true>(ka, kbr, ring, ub, fp, ya);
      STAMP(13);
      bar();
      STAMP(5);
      for (int st = 1; st < 4; st++) {
        ub += 1024;
        near_stage<false>(ka, kbr, ring, ub, OFF_RY + (st & 1) * 12288 + c * 2048 + lane * 16, OFF_YS + (st & 1) * 6144 + c * 1024 + la * 32 + lg * 8);
        if (st == 1) STAMP(14);
        bar();
        if (st == 1) STAMP(6); if (st == 2) STAMP(7); if (st == 3) STAMP(8);
      }
    } else {
      for (int i = 0; i < 5; i++) bar();
    }
  } else {
    u4 t2[3][2], t3[4];
    const bool act = c < nch;
    u4 ua0, uc0, ua1, uc1;
    if (act) {
      const int kf = OFF_KF + c * KFS;
      f2 v3 = sum3_2(part + 2048 + pslot(lane >> 5, lane & 15) + ((lane >> 4) & 1) * 8);
      f4 v2 = sum3_4(part + 1024 + pslot(2 + ((lane >> 4) & 1), lane & 15));
      float k2048 = sum3_1(part + 2048 + pslot(2, 0));
      ua0 = *LDSP(u4, ubase - 32); uc0 = *LDSP(u4, ubase - 48); ua1 = *LDSP(u4, ubase + 512 - 32); uc1 = *LDSP(u4, ubase + 512 - 48);
      float n3 = nextlane(v3.x), n2 = nextlane(v2.x);
      if (lane == 63) n3 = k2048;
      if (lane == 31) n2 = 0.f;
      *LDSP(unsigned, kf + 512 + (63 - lane) * 4) = pk2(v3.y, n3);
      if (lane < 16) *LDSP(unsigned, kf + 512 + (64 + lane) * 4) = 0u;
      {
        int x = lane < 32 ? 47 - lane : (lane < 48 ? lane - 32 : lane);
        bool nz = lane < 32;
        *LDSP(unsigned, kf + x * 4) = nz ? pk2(v2.y, v2.z) : 0u;
        *LDSP(unsigned, kf + 256 + x * 4) = nz ? pk2(v2.w, n2) : 0u;
      }
      int a = kf + (15 - la + 4 * lg) * 4;
      asm volatile("" : "+v"(a));
#pragma unroll
      for (int d = 0; d < 3; d++)
#pragma unroll
        for (int h = 0; h < 2; h++)
#pragma unroll
          for (int j = 0; j < 4; j++) t2[d][h][j] = *LDSP(unsigned, a + h * 256 + (32 - 16 * d + j) * 4);
#pragma unroll
      for (int d = 0; d < 4; d++)
#pragma unroll
        for (int j = 0; j < 4; j++) t3[d][j] = *LDSP(unsigned, a + 512 + (48 - 16 * d + j) * 4);
    }
    STAMP(12);
    bar();
    STAMP(4);
    u4 w2[6][2], w3[4];
#pragma unroll
    for (int st = 0; st < 4; st++) {
      if (st > 0) {
        for (int rw = c; rw < 8; rw += 6) store_rows(outp, six, 512 * (st - 1) + 64 * rw + lane, dk01, dk23, dk45);
      }
      STAMP(13 + 2 * st);
      if (st < 3 && act) {
#pragma unroll
        for (int h = 0; h < 2; h++) {
          const int be = 2 * st + h;
          const u4 ua = h ? ua1 : ua0, uc = h ? uc1 : uc0;
          f4 q0 = mfma(ua, cst[0], zero4), q1 = mfma(ua, cst[1], zero4), q2 = mfma(ua, cst[2], zero4), q3 = mfma(uc, cst[3], zero4);
          f4 q4, q5;
          if (be < 4) { q4 = mfma(ua, cst[4], zero4); q5 = mfma(uc, cst[5], zero4); }
#pragma unroll
          for (int j = 0; j < 4; j++) { w2[be][0][j] = pk2(q0[j], q1[j]); w2[be][1][j] = pk2(q2[j], q3[j]); }
          if (be < 4) {
#pragma unroll
            for (int j = 0; j < 4; j++) w3[be][j] = pk2(q4[j], q5[j]);
          }
        }
        if (st < 2) {
          const int nb = ubase + 1024 * (st + 1);
          ua0 = *LDSP(u4, nb - 32); uc0 = *LDSP(u4, nb - 48); ua1 = *LDSP(u4, nb + 512 - 32); uc1 = *LDSP(u4, nb + 512 - 48);
        }
        const int fp = OFF_RY + ((st + 1) & 1) * 12288 + c * 2048 + lane * 16;
        f4 acc[2] = {zero4, zero4};
#pragma unroll
        for (int d = 7; d >= 4; d--)
#pragma unroll
          for (int h = 0; h < 2; h++) { const int q = 2 * (st + 1) + h; if (q - d >= 0) acc[h] = mfma(w3[q - d], t3[d - 4], acc[h]); }
#pragma unroll
        for (int d = 4; d >= 2; d--)
#pragma unroll
          for (int e = 0; e < 2; e++)
#pragma unroll
            for (int h = 0; h < 2; h++) { const int q = 2 * (st + 1) + h; if (q - d >= 0) acc[h] = mfma(w2[q - d][e], t2[d - 2][e], acc[h]); }
        *LDSP(f4, fp) = acc[0];
        *LDSP(f4, fp + 1024) = acc[1];
      }
      STAMP(14 + 2 * st);
      bar();
      STAMP(5 + st);
    }
  }
  if (w < 8) store_rows(outp, six, 1536 + 64 * w + lane, dk01, dk23, dk45);
  STAMP(9);
  STAMP_DUMP;
}

extern "C" void kernel_launch(void* const* d_in, const int* in_sizes, int n_in,
                              void* d_out, int out_size, void* d_ws, size_t ws_size,
                              hipStream_t stream) {
  const char* const* in = (const char* const*)d_in;
  Params p;
  p.b[0] = Br{in[0], in[1], in[2], in[3], in[4], in[3], in[4], in[5], in[6]};
  p.b[1] = Br{in[7], in[8], in[9], in[12], in[13], in[10], in[11], in[14], in[15]};
  p.out = (char*)d_out;
  p.dbg = (unsigned long long*)d_ws;
  ssm_fused<<<240, 768, 0, stream>>>(p);
}
```

```cpp
#define PROBE 99
#define WBL2 if (w == 0) { __builtin_amdgcn_fence(__ATOMIC_RELEASE, ""); asm volatile("s_waitcnt vmcnt(0)" ::: "memory"); }
#define SPINSEL true
#include <hip/hip_runtime.h>
#include <hip/hip_bf16.h>
#include <stdint.h>

typedef float f2 __attribute__((ext_vector_type(2)));
typedef float f4 __attribute__((ext_vector_type(4)));
typedef __bf16 bf2 __attribute__((ext_vector_type(2)));
typedef __bf16 bf8 __attribute__((ext_vector_type(8)));
typedef unsigned u2 __attribute__((ext_vector_type(2)));
typedef unsigned u3 __attribute__((ext_vector_type(3)));
typedef unsigned u4 __attribute__((ext_vector_type(4)));
typedef unsigned short u16;
#define DI __device__ __forceinline__
#define LDSP(T, off) ((__attribute__((address_space(3))) T*)(unsigned long)(unsigned)(off))
#define CSTP(T, ptr) ((const __attribute__((address_space(4))) T*)(unsigned long)(ptr))
#define SB() __builtin_amdgcn_sched_barrier(0)

#define STAMP_DECL
#define STAMP(i) do { if ((i) == PROBE) { __builtin_amdgcn_sched_barrier(0); asm volatile("s_memrealtime %0\n\ts_waitcnt lgkmcnt(0)" : "=s"(rtk_) :: "memory"); __builtin_amdgcn_sched_barrier(0);} } while (0)
#define STAMP_DUMP do { asm volatile("s_waitcnt vmcnt(0)" ::: "memory"); bar(); STAMP(30); WBL2; if (w == 0 && (SPINSEL)) { unsigned long long tgt = rt0_ + 3000ull + 10ull * (rtk_ - rt0_); \
   for (int it_ = 0; it_ < 200000; it_++) { if (__builtin_amdgcn_s_memrealtime() >= tgt) break; __builtin_amdgcn_s_sleep(1); } } } while (0)

#define CODE_PF_WAVES 3

constexpr int NMODE = 192;
constexpr int SCR = 16 * 288;
constexpr int KNC = 1216;
constexpr int OFF_KN = 0;
constexpr int OFF_KF = 6 * 4 * KNC;
constexpr int KFS = 896;
constexpr int OFF_YS = OFF_KF + 6 * KFS;
constexpr int OFF_RY = 12 * SCR;
constexpr int UT_PAD = 528;
constexpr int UTS = (UT_PAD + 2048) * 2;
constexpr int OFF_UT = OFF_RY + 18 * 3072;
constexpr int LDS_BYTES = OFF_UT + 6 * UTS;
static_assert(OFF_YS + 12288 <= OFF_RY, "lds alias");

#define CM_MUL(d, a, b) "v_pk_mul_f32 " d ", " a ", " b " op_sel_hi:[0,1]\n\t"
#define CM_FMA(d, a, b) "v_pk_fma_f32 " d ", " a ", " b ", " d " op_sel:[1,1,0] op_sel_hi:[1,0,1] neg_lo:[0,1,0]\n\t"
#define CC_MUL(d, a, b) "v_pk_mul_f32 " d ", " a ", " b " op_sel_hi:[0,1] neg_hi:[0,1]\n\t"
#define CC_FMA(d, a, b) "v_pk_fma_f32 " d ", " a ", " b ", " d " op_sel:[1,1,0] op_sel_hi:[1,0,1]\n\t"
DI f2 cmul(f2 a, f2 b) {
  f2 r;
  asm(CM_MUL("%0", "%1", "%2") CM_FMA("%0", "%1", "%2") : "=&v"(r) : "v"(a), "v"(b));
  return r;
}
DI void cmul2(f2& r0, f2& r1, f2 a0, f2 b0, f2 a1, f2 b1) {
  asm(CM_MUL("%0", "%2", "%3") CM_MUL("%1", "%4", "%5") CM_FMA("%0", "%2", "%3") CM_FMA("%1", "%4", "%5")
      : "=&v"(r0), "=&v"(r1) : "v"(a0), "v"(b0), "v"(a1), "v"(b1));
}
DI void cmul3(f2& r0, f2& r1, f2& r2, f2 a, f2 b0, f2 b1, f2 b2) {
  asm(CM_MUL("%0", "%3", "%4") CM_MUL("%1", "%3", "%5") CM_MUL("%2", "%3", "%6")
      CM_FMA("%0", "%3", "%4") CM_FMA("%1", "%3", "%5") CM_FMA("%2", "%3", "%6")
      : "=&v"(r0), "=&v"(r1), "=&v"(r2) : "v"(a), "v"(b0), "v"(b1), "v"(b2));
}
DI void cmul4(f2& r0, f2& r1, f2& r2, f2& r3, f2 a, f2 b0, f2 b1, f2 b2, f2 b3) {
  asm(CM_MUL("%0", "%4", "%5") CM_MUL("%1", "%4", "%6") CM_MUL("%2", "%4", "%7") CM_MUL("%3", "%4", "%8")
      CM_FMA("%0", "%4", "%5") CM_FMA("%1", "%4", "%6") CM_FMA("%2", "%4", "%7") CM_FMA("%3", "%4", "%8")
      : "=&v"(r0), "=&v"(r1), "=&v"(r2), "=&v"(r3) : "v"(a), "v"(b0), "v"(b1), "v"(b2), "v"(b3));
}
DI void cmulc4(f2& r0, f2& r1, f2& r2, f2& r3, f2 a0, f2 a1, f2 a2, f2 a3, f2 b) {
  asm(CC_MUL("%0", "%4", "%8") CC_MUL("%1", "%5", "%8") CC_MUL("%2", "%6", "%8") CC_MUL("%3", "%7", "%8")
      CC_FMA("%0", "%4", "%8") CC_FMA("%1", "%5", "%8") CC_FMA("%2", "%6", "%8") CC_FMA("%3", "%7", "%8")
      : "=&v"(r0), "=&v"(r1), "=&v"(r2), "=&v"(r3) : "v"(a0), "v"(a1), "v"(a2), "v"(a3), "v"(b));
}
DI void cmulc_sq(f2& r0, f2& r1, f2 a, f2 b) {
  asm(CC_MUL("%0", "%2", "%3") CM_MUL("%1", "%3", "%3") CC_FMA("%0", "%2", "%3") CM_FMA("%1", "%3", "%3")
      : "=&v"(r0), "=&v"(r1) : "v"(a), "v"(b));
}
DI void cmulc2_sq(f2& r0, f2& r1, f2& r2, f2 a0, f2 a1, f2 b) {
  asm(CC_MUL("%0", "%3", "%5") CC_MUL("%1", "%4", "%5") CM_MUL("%2", "%5", "%5")
      CC_FMA("%0", "%3", "%5") CC_FMA("%1", "%4", "%5") CM_FMA("%2", "%5", "%5")
      : "=&v"(r0), "=&v"(r1), "=&v"(r2) : "v"(a0), "v"(a1), "v"(b));
}
DI unsigned pk2(float lo, float hi) {
  bf2 r = __builtin_convertvector((f2{lo, hi}), bf2);
  return __builtin_bit_cast(unsigned, r);
}
DI unsigned pk2(f2 a) { return pk2(a.x, a.y); }
DI float bfhi(unsigned x) { return __uint_as_float(x & 0xffff0000u); }
DI float bflo(unsigned x) { return __uint_as_float(x << 16); }
DI float rbf(float f) { return __uint_as_float(pk2(f, 0.f) << 16); }
DI f4 mfma(u4 a, u4 b, f4 c) {
  return __builtin_amdgcn_mfma_f32_16x16x32_bf16(__builtin_bit_cast(bf8, a), __builtin_bit_cast(bf8, b), c, 0, 0, 0);
}
DI void bar() { asm volatile("s_waitcnt lgkmcnt(0)\n\ts_barrier" ::: "memory"); }
template <int R0>
DI void wr4(int m0v, unsigned a, unsigned b, unsigned c, unsigned d) {
  asm volatile("s_mov_b32 m0, %0\n\ts_nop 0\n\t"
               "ds_write_addtid_b32 %1 offset:%5\n\tds_write_addtid_b32 %2 offset:%6\n\t"
               "ds_write_addtid_b32 %3 offset:%7\n\tds_write_addtid_b32 %4 offset:%8"
               :: "s"(m0v), "v"(a), "v"(b), "v"(c), "v"(d),
                  "i"(R0 * 288), "i"((R0 + 1) * 288), "i"((R0 + 2) * 288), "i"((R0 + 3) * 288) : "memory");
}
DI f2 cexp_dir(float a2, float b2) {
  float m = __builtin_amdgcn_exp2f(a2);
  float ph = __builtin_amdgcn_fractf(b2);
  return f2{m * __builtin_amdgcn_cosf(ph), m * __builtin_amdgcn_sinf(ph)};
}
DI float exp_acc(float x) {
  float h = x * 1.44269504088896341f;
  float l = fmaf(x, 1.92596299112661746e-8f, fmaf(x, 1.44269504088896341f, -h));
  float e = __builtin_amdgcn_exp2f(h);
  return fmaf(e * l, 0.69314718055994531f, e);
}
DI float nextlane(float v) {
  return __int_as_float(__builtin_amdgcn_update_dpp(0, __float_as_int(v), 0x130, 0xf, 0xf, true));
}
DI float first(float v) { return __int_as_float(__builtin_amdgcn_readfirstlane(__float_as_int(v))); }
DI int pslot(int g, int a) { return 16 * (16 * g + ((a + 4 * g) & 15)); }

struct Br { const char *u, *are, *aim, *cre, *cim, *bre, *bim, *ldt, *dsk; };
struct Params { Br b[2]; char* out; unsigned long long* dbg; };
struct Raw { unsigned ar, ai, cr, ci, br, bi; };

DI void read4(int ra, u4 (&f)[4]) {
#pragma unroll
  for (int s = 0; s < 4; s++) f[s] = *LDSP(u4, ra + 64 * s);
}

DI void kgen_task(const Raw& r, float ldt, int br, int scr, int ra, int pa) {
  float dt = rbf(exp_acc(ldt));
  float ar, cx, cy;
  if (br == 0) {
    ar = -rbf(exp_acc(bflo(r.ar)));
    float d2 = dt + dt;
    cx = bflo(r.cr) * d2; cy = bflo(r.ci) * d2;
  } else {
    ar = bflo(r.ar);
    float bx = bflo(r.br) * dt, by = bflo(r.bi) * dt, c0 = bflo(r.cr), c1 = bflo(r.ci);
    cx = c0 * bx - c1 * by; cy = c0 * by + c1 * bx;
  }
  float a2 = (ar * dt) * 1.44269504088896341f;
  float b2 = (bflo(r.ai) * dt) * 0.15915494309189535f;
  u4 fa[4], fx[4];
  const f4 zero4 = {0.f, 0.f, 0.f, 0.f};
  f4 d0 = zero4, d1 = zero4, d2 = zero4;
  f2 z1 = cexp_dir(a2, b2);
  f2 z256 = cexp_dir(a2 * 256.f, b2 * 256.f);
  f2 z2, z3, z4, z5, z6, z7, z8, z9, z10, z11, z12, z13, z14, z15, w, w2, w4, z512, z768, z1024, z1280, z1536, z1792, z2048;
  cmul2(z2, z512, z1, z1, z256, z256);
  cmul2(z3, z4, z2, z1, z2, z2);
  wr4<0>(scr, 0x00003f80u, pk2(z1), pk2(z2), pk2(z3));
  cmul4(z8, z5, z6, z7, z4, z4, z1, z2, z3);
  wr4<4>(scr, pk2(z4), pk2(z5), pk2(z6), pk2(z7));
  cmul4(w, z9, z10, z11, z8, z8, z1, z2, z3);
  wr4<8>(scr, pk2(z8), pk2(z9), pk2(z10), pk2(z11));
  f2 x0 = f2{cx, -cy}, x1, x2, x3;
  cmulc_sq(x1, w2, x0, w);
  cmul4(z12, z13, z14, z15, z8, z4, z5, z6, z7);
  wr4<12>(scr, pk2(z12), pk2(z13), pk2(z14), pk2(z15));
  read4(ra, fa);
  cmulc2_sq(x2, x3, w4, x0, x1, w2);
  wr4<0>(scr, pk2(x0), pk2(x1), pk2(x2), pk2(x3));
  f2 y0, y1, y2, y3;
  cmulc4(y0, y1, y2, y3, x0, x1, x2, x3, w4);
  wr4<4>(scr, pk2(y0), pk2(y1), pk2(y2), pk2(y3));
  cmulc4(x0, x1, x2, x3, y0, y1, y2, y3, w4);
  wr4<8>(scr, pk2(x0), pk2(x1), pk2(x2), pk2(x3));
  cmulc4(y0, y1, y2, y3, x0, x1, x2, x3, w4);
  wr4<12>(scr, pk2(y0), pk2(y1), pk2(y2), pk2(y3));
  read4(ra, fx);
  cmul4(z768, y1, y2, y3, z256, z512, z2, z4, z6);
  wr4<0>(scr, pk2(z256), pk2(y1), pk2(y2), pk2(y3));
  cmul4(y0, y1, y2, y3, z256, z8, z10, z12, z14);
  wr4<4>(scr, pk2(y0), pk2(y1), pk2(y2), pk2(y3));
#pragma unroll
  for (int s = 0; s < 4; s++) d0 = mfma(fa[s], fx[s], d0);
  cmul4(z1024, y1, y2, y3, z512, z512, z4, z8, z12);
  wr4<8>(scr, pk2(z512), pk2(y1), pk2(y2), pk2(y3));
  cmul4(z1536, y1, y2, y3, z768, z768, z4, z8, z12);
  wr4<12>(scr, pk2(z768), pk2(y1), pk2(y2), pk2(y3));
  read4(ra, fa);
  cmul4(z1280, z1792, z2048, y0, z1024, z256, z768, z1024, z8);
  cmul3(y1, y2, y3, z8, z1280, z1536, z1792);
  unsigned t0 = pk2(z1024), t1 = pk2(y0), t2 = pk2(z1280), t3 = pk2(y1), t4 = pk2(z1536), t5 = pk2(y2), t6 = pk2(z1792), t7 = pk2(y3), t8 = pk2(z2048);
#pragma unroll
  for (int s = 0; s < 4; s++) d1 = mfma(fa[s], fx[s], d1);
  wr4<0>(scr, t0, t1, t2, t3);
  wr4<4>(scr, t4, t5, t6, t7);
  asm volatile("s_mov_b32 m0, %0\n\ts_nop 0\n\tds_write_addtid_b32 %1 offset:%2" :: "s"(scr), "v"(t8), "i"(8 * 288) : "memory");
  read4(ra, fa);
  *LDSP(f4, pa) = d0;
  *LDSP(f4, pa + 1024) = d1;
#pragma unroll
  for (int s = 0; s < 4; s++) d2 = mfma(fa[s], fx[s], d2);
  *LDSP(f4, pa + 2048) = d2;
}

DI f4 sum3_4(int a) { return *LDSP(f4, a) + *LDSP(f4, a + 3072) + *LDSP(f4, a + 6144); }
DI f2 sum3_2(int a) { return *LDSP(f2, a) + *LDSP(f2, a + 3072) + *LDSP(f2, a + 6144); }
DI float sum3_1(int a) { return *LDSP(float, a) + *LDSP(float, a + 3072) + *LDSP(float, a + 6144); }

DI void put_unit(int a, float v0, float v1, float v2, float v3, float v4, float v5, float v6) {
  unsigned p10 = pk2(v1, v0), p21 = pk2(v2, v1), p32 = pk2(v3, v2), p43 = pk2(v4, v3), p54 = pk2(v5, v4), p65 = pk2(v6, v5);
  *LDSP(u2, a) = u2{p32, p10};
  *LDSP(u2, a + KNC) = u2{p43, p21};
  *LDSP(u2, a + 2 * KNC) = u2{p54, p32};
  *LDSP(u2, a + 3 * KNC) = u2{p65, p43};
}

DI u4 band(int lane, int lo, int n) {
  unsigned m = (((1u << n) - 1u) << lo << (lane & 15)) >> (8 * (lane >> 4));
  u4 r;
#pragma unroll
  for (int j = 0; j < 4; j++)
    r[j] = ((unsigned)__builtin_amdgcn_sbfe(m, 2 * j, 1) & 0xffffu) | ((unsigned)__builtin_amdgcn_sbfe(m, 2 * j + 1, 1) << 16);
  return r;
}
DI u4 band1(int lane, int lo, int n) { u4 r = band(lane, lo, n) & 0x3f803f80u; asm volatile("" : "+v"(r)); return r; }

DI void store_rows(char* outp, bool six, int t, unsigned dk01, unsigned dk23, unsigned dk45) {
  const int ya = OFF_YS + ((t >> 9) & 1) * 6144 + (t & 511) * 2;
  const int ua = OFF_UT + (UT_PAD + t) * 2;
  unsigned y[6], u[6];
#pragma unroll
  for (int c = 0; c < 6; c++) { y[c] = *LDSP(u16, ya + c * 1024); u[c] = *LDSP(u16, ua + c * UTS); }
  unsigned s01 = pk2(bflo(u[0]) * bflo(dk01), bflo(u[1]) * bfhi(dk01));
  unsigned s23 = pk2(bflo(u[2]) * bflo(dk23), bflo(u[3]) * bfhi(dk23));
  unsigned s45 = pk2(bflo(u[4]) * bflo(dk45), bflo(u[5]) * bfhi(dk45));
  unsigned o01 = pk2(bflo(y[0]) + bflo(s01), bflo(y[1]) + bfhi(s01));
  unsigned o23 = pk2(bflo(y[2]) + bflo(s23), bflo(y[3]) + bfhi(s23));
  unsigned o45 = pk2(bflo(y[4]) + bflo(s45), bflo(y[5]) + bfhi(s45));
  char* dst = outp + (unsigned)t * 2560u;
  if (six) *(u3*)dst = u3{o01, o23, o45};
  else *(u2*)dst = u2{o01, o23};
}

template <bool FIRST>
DI void near_stage(const u4 (&ka)[17], const u4& kbr, u4 (&ring)[9], int ub, int fp, int ya) {
  f4 a0 = {0.f, 0.f, 0.f, 0.f}, a1 = a0;
  if (!FIRST) { a0 = *LDSP(f4, fp); a1 = *LDSP(f4, fp + 1024); }
  SB();
#pragma unroll
  for (int n = 0; n < 27; n++) {
    const u4 v = ring[n % 9];
    if (n < 17) a1 = mfma(ka[n], v, a1);
    if (n >= 8 && n < (FIRST ? 17 : 25)) a0 = mfma(ka[n - 8], v, a0);
    if (!FIRST && n == 25) a0 = mfma(kbr, v, a0);
    if (!FIRST && n == 26) a1 = mfma(kbr, v, a1);
    const int m = n + 9;
    if (m < 25) { if (!FIRST || m < 17) ring[n % 9] = *LDSP(u4, ub + 1600 - 64 * m); }
    else if (m < 27) { if (!FIRST) ring[n % 9] = *LDSP(u4, ub + 1600 - 480 - 2064 + 512 * (m - 25)); }
    else ring[n % 9] = *LDSP(u4, ub + 1600 + 1024 - 64 * (m - 27));
    SB();
  }
  *LDSP(u2, ya) = u2{pk2(a0[0], a0[1]), pk2(a0[2], a0[3])};
  *LDSP(u2, ya + 512) = u2{pk2(a1[0], a1[1]), pk2(a1[2], a1[3])};
}

__global__ __launch_bounds__(768, 3) void ssm_fused(Params p) {
  unsigned long long rt0_, rtk_;
  asm volatile("s_memrealtime %0" : "=s"(rt0_) :: "memory");
  __builtin_amdgcn_sched_barrier(0);
  rtk_ = rt0_;
  __shared__ __attribute__((aligned(16))) char smem[LDS_BYTES];
  asm volatile("" :: "v"((__attribute__((address_space(3))) char*)smem));
  const int tid = threadIdx.x, lane = tid & 63;
  const int w = __builtin_amdgcn_readfirstlane(tid >> 6);
  const int bx = blockIdx.x & 7, bj = blockIdx.x >> 3;
  const int blk = bx * 5 + bj / 6, sl = bj % 6;
  const bool six = sl < 4;
  const int nch = six ? 6 : 4;
  const int ch0 = blk * 32 + (six ? 6 * sl : 8 + 4 * sl);
  const int br = blk >= 20;
  const int e0 = ch0 - br * 640;
  STAMP(28);
  const Br B = p.b[br];
  STAMP_DECL
  STAMP(0);

  asm volatile("" :: "s"(B.are), "s"(B.dsk));
  STAMP(29);
  const bool two = six && w < 6;
  const int tk1 = two ? w + 12 : w;
  const unsigned o0 = (e0 * NMODE + w * 64 + lane) * 2u, o1 = (e0 * NMODE + tk1 * 64 + lane) * 2u;
  Raw r0, r1;
  r0.ar = *(const u16*)(B.are + o0); r0.ai = *(const u16*)(B.aim + o0); r0.cr = *(const u16*)(B.cre + o0);
  r0.ci = *(const u16*)(B.cim + o0); r0.br = *(const u16*)(B.bre + o0); r0.bi = *(const u16*)(B.bim + o0);
  unsigned pf0, pf1 = 0, pf2 = 0;
  {
    unsigned row = bj * 69 + tid / 5;
    row = row < 2047u ? row : 2047u;
    unsigned off = row * 1280u + (bx & 3) * 320u + (tid % 5) * 64u;
    if (tid >= 345) off = e0 * 2u;
    asm volatile("global_load_dword %0, %1, %2" : "=v"(pf0) : "v"(off), "s"(B.u) : "memory");
    if (w >= 12 - CODE_PF_WAVES) {
      unsigned long long pc;
      asm volatile("s_getpc_b64 %0" : "=s"(pc));
      unsigned co = ((11 - w) * 64 + lane) * 64u;
      asm volatile("global_load_dword %0, %1, %2" : "=v"(pf1) : "v"(co), "s"(pc & ~63ull) : "memory");
    }
    if (w == 6) {
      unsigned oo = lane * 81920u + ch0 * 2u;
      asm volatile("global_load_dword %0, %1, %2" : "=v"(pf2) : "v"(oo), "s"(p.out) : "memory");
    }
  }
  r1.ar = *(const u16*)(B.are + o1); r1.ai = *(const u16*)(B.aim + o1); r1.cr = *(const u16*)(B.cre + o1);
  r1.ci = *(const u16*)(B.cim + o1); r1.br = *(const u16*)(B.bre + o1); r1.bi = *(const u16*)(B.bim + o1);
  const int c0 = w / 3, c1 = tk1 / 3;
  const unsigned l0 = CSTP(unsigned, B.ldt)[(e0 + c0) >> 1], l1 = CSTP(unsigned, B.ldt)[(e0 + c1) >> 1];
  const unsigned dk01 = CSTP(unsigned, B.dsk)[e0 >> 1], dk23 = CSTP(unsigned, B.dsk)[(e0 >> 1) + 1],
                 dk45 = CSTP(unsigned, B.dsk)[(e0 >> 1) + (six ? 2 : 1)];
  SB();
  if (tid < 6 * 66) *LDSP(u4, OFF_UT + (tid / 66) * UTS + (tid % 66) * 16) = u4{0, 0, 0, 0};
  u4 cst[6];
  if (w >= 6) {
    cst[0] = band1(lane, 16 - 4 - 1, 3);
    cst[1] = band1(lane, 16 - 8 - 2, 5);
    cst[2] = band1(lane, 16 - 12 - 1, 3);
    cst[3] = band1(lane, 24 - 16 - 2, 5);
    cst[4] = band1(lane, 16 - 8 - 3, 7);
    cst[5] = band1(lane, 24 - 16 - 4, 9);
  } else {
    cst[0] = band(lane, 4, 7);
    asm volatile("" : "+v"(cst[0]));
  }
  asm volatile("s_waitcnt vmcnt(0)" : "+v"(pf0), "+v"(pf1), "+v"(pf2) :: "memory");
  STAMP(1);
  u3 ur[4];
  if (w >= 4) {
    const unsigned so = (tid - 256) * 5120u + e0 * 2u - (six ? 0u : 4u);
#pragma unroll
    for (int r = 0; r < 4; r++) ur[r] = *(const u3*)(B.u + (so + r * 1280u));
  }
  {
    const int scr = w * SCR;
    const int ra = scr + (lane & 15) * 288 + (lane >> 4) * 16;
    int pa = OFF_RY + w * 3072 + pslot(lane >> 4, lane & 15);
    float ldt = (c0 & 1) ? bfhi(l0) : bflo(l0);
    for (int it = 0;; it++) {
      kgen_task(r0, ldt, br, scr, ra, pa);
      if (it == 1 || !two) break;
      r0 = r1; ldt = (c1 & 1) ? bfhi(l1) : bflo(l1); pa += 12 * 3072;
    }
  }
  STAMP(2);
  if (w >= 4) {
    const int ua = OFF_UT + (UT_PAD + (tid - 256) * 4) * 2 - (six ? 0 : 2 * UTS);
#pragma unroll
    for (int d = 0; d < 3; d++) {
      if (d > 0 || six) {
        *LDSP(u2, ua + (2 * d) * UTS) = u2{__builtin_amdgcn_perm(ur[1][d], ur[0][d], 0x05040100u), __builtin_amdgcn_perm(ur[3][d], ur[2][d], 0x05040100u)};
        *LDSP(u2, ua + (2 * d + 1) * UTS) = u2{__builtin_amdgcn_perm(ur[1][d], ur[0][d], 0x07060302u), __builtin_amdgcn_perm(ur[3][d], ur[2][d], 0x07060302u)};
      }
    }
  }
  const int c = w < 6 ? w : w - 6;
  const int la = lane & 15, lg = lane >> 4;
  const int part = OFF_RY + c * 3 * 3072;
  const int ubase = OFF_UT + c * UTS + UT_PAD * 2 + la * 32 + lg * 16;
  char* outp = p.out + ch0 * 2;
  const f4 zero4 = {0.f, 0.f, 0.f, 0.f};
  bar();
  STAMP(3);

  if (w < 6) {
    if (c < nch) {
      u4 ka[17], kbr, ring[9];
      const int kn = OFF_KN + c * 4 * KNC;
      f4 va = sum3_4(part + pslot(lane & 3, lane >> 2));
      f2 vb = sum3_2(part + 1024 + pslot((lane >> 1) & 1, lane >> 2) + (lane & 1) * 8);
      float k512 = sum3_1(part + 1024 + pslot(2, 0));
      float k1024 = sum3_1(part + 2048 + pslot(0, 0));
      int ub = ubase + 480 - 1600;
#pragma unroll
      for (int n = 0; n < 9; n++) ring[n] = *LDSP(u4, ub + 1600 - 64 * n);
      float k256 = first(vb.x), k258 = first(vb.y), k0 = first(va.x), k1 = first(va.y), k2 = first(va.z);
      float n0 = nextlane(va.x), n1 = nextlane(va.y), n2 = nextlane(va.z);
      float ne = nextlane(vb.x), no = nextlane(vb.y);
      if (lane == 63) { n0 = k256; n1 = k256; n2 = k258; ne = k512; no = k512; }
      STAMP(10);
      put_unit(kn + (135 - lane) * 8, va.x, va.y, va.z, va.w, n0, n1, n2);
      put_unit(kn + (71 - lane) * 8, vb.x, vb.x, vb.y, ne, ne, ne, no);
      if (lane < 13) {
        int i = lane < 5 ? lane - 5 : 123 + lane;
        float a = (i == 128) ? k512 : 0.f;
        bool m1 = (i == -1);
        put_unit(kn + (135 - i) * 8, a, a, a, 0.f, m1 ? k0 : 0.f, m1 ? k1 : 0.f, m1 ? k2 : 0.f);
      }
      STAMP(11);
      const int s = (la + 1) & 3;
      const int kr = kn + s * KNC + (543 - (la - 8 * lg + 16) + s) * 2 - 1024;
#pragma unroll
      for (int m = 0; m < 17; m++) {
        u2 lo = *LDSP(u2, kr + 1024 - 64 * m), hi = *LDSP(u2, kr + 1024 - 64 * m + 8);
        ka[m] = u4{lo.x, lo.y, hi.x, hi.y};
      }
      kbr = cst[0] & pk2(k1024, k1024);
      int fp = OFF_RY + c * 2048 + lane * 16;
      int ya = OFF_YS + c * 1024 + la * 32 + lg * 8;
      STAMP(12);
      bar();
      STAMP(4);
      near_stage<true>(ka, kbr, ring, ub, fp, ya);
      STAMP(13);
      bar();
      STAMP(5);
      for (int st = 1; st < 4; st++) {
        ub += 1024;
        near_stage<false>(ka, kbr, ring, ub, OFF_RY + (st & 1) * 12288 + c * 2048 + lane * 16, OFF_YS + (st & 1) * 6144 + c * 1024 + la * 32 + lg * 8);
        if (st == 1) STAMP(14);
        bar();
        if (st == 1) STAMP(6); if (st == 2) STAMP(7); if (st == 3) STAMP(8);
      }
    } else {
      for (int i = 0; i < 5; i++) bar();
    }
  } else {
    u4 t2[3][2], t3[4];
    const bool act = c < nch;
    u4 ua0, uc0, ua1, uc1;
    if (act) {
      const int kf = OFF_KF + c * KFS;
      f2 v3 = sum3_2(part + 2048 + pslot(lane >> 5, lane & 15) + ((lane >> 4) & 1) * 8);
      f4 v2 = sum3_4(part + 1024 + pslot(2 + ((lane >> 4) & 1), lane & 15));
      float k2048 = sum3_1(part + 2048 + pslot(2, 0));
      ua0 = *LDSP(u4, ubase - 32); uc0 = *LDSP(u4, ubase - 48); ua1 = *LDSP(u4, ubase + 512 - 32); uc1 = *LDSP(u4, ubase + 512 - 48);
      float n3 = nextlane(v3.x), n2 = nextlane(v2.x);
      if (lane == 63) n3 = k2048;
      if (lane == 31) n2 = 0.f;
      *LDSP(unsigned, kf + 512 + (63 - lane) * 4) = pk2(v3.y, n3);
      if (lane < 16) *LDSP(unsigned, kf + 512 + (64 + lane) * 4) = 0u;
      {
        int x = lane < 32 ? 47 - lane : (lane < 48 ? lane - 32 : lane);
        bool nz = lane < 32;
        *LDSP(unsigned, kf + x * 4) = nz ? pk2(v2.y, v2.z) : 0u;
        *LDSP(unsigned, kf + 256 + x * 4) = nz ? pk2(v2.w, n2) : 0u;
      }
      int a = kf + (15 - la + 4 * lg) * 4;
      asm volatile("" : "+v"(a));
#pragma unroll
      for (int d = 0; d < 3; d++)
#pragma unroll
        for (int h = 0; h < 2; h++)
#pragma unroll
          for (int j = 0; j < 4; j++) t2[d][h][j] = *LDSP(unsigned, a + h * 256 + (32 - 16 * d + j) * 4);
#pragma unroll
      for (int d = 0; d < 4; d++)
#pragma unroll
        for (int j = 0; j < 4; j++) t3[d][j] = *LDSP(unsigned, a + 512 + (48 - 16 * d + j) * 4);
    }
    STAMP(12);
    bar();
    STAMP(4);
    u4 w2[6][2], w3[4];
#pragma unroll
    for (int st = 0; st < 4; st++) {
      if (st > 0) {
        for (int rw = c; rw < 8; rw += 6) store_rows(outp, six, 512 * (st - 1) + 64 * rw + lane, dk01, dk23, dk45);
      }
      STAMP(13 + 2 * st);
      if (st < 3 && act) {
#pragma unroll
        for (int h = 0; h < 2; h++) {
          const int be = 2 * st + h;
          const u4 ua = h ? ua1 : ua0, uc = h ? uc1 : uc0;
          f4 q0 = mfma(ua, cst[0], zero4), q1 = mfma(ua, cst[1], zero4), q2 = mfma(ua, cst[2], zero4), q3 = mfma(uc, cst[3], zero4);
          f4 q4, q5;
          if (be < 4) { q4 = mfma(ua, cst[4], zero4); q5 = mfma(uc, cst[5], zero4); }
#pragma unroll
          for (int j = 0; j < 4; j++) { w2[be][0][j] = pk2(q0[j], q1[j]); w2[be][1][j] = pk2(q2[j], q3[j]); }
          if (be < 4) {
#pragma unroll
            for (int j = 0; j < 4; j++) w3[be][j] = pk2(q4[j], q5[j]);
          }
        }
        if (st < 2) {
          const int nb = ubase + 1024 * (st + 1);
          ua0 = *LDSP(u4, nb - 32); uc0 = *LDSP(u4, nb - 48); ua1 = *LDSP(u4, nb + 512 - 32); uc1 = *LDSP(u4, nb + 512 - 48);
        }
        const int fp = OFF_RY + ((st + 1) & 1) * 12288 + c * 2048 + lane * 16;
        f4 acc[2] = {zero4, zero4};
#pragma unroll
        for (int d = 7; d >= 4; d--)
#pragma unroll
          for (int h = 0; h < 2; h++) { const int q = 2 * (st + 1) + h; if (q - d >= 0) acc[h] = mfma(w3[q - d], t3[d - 4], acc[h]); }
#pragma unroll
        for (int d = 4; d >= 2; d--)
#pragma unroll
          for (int e = 0; e < 2; e++)
#pragma unroll
            for (int h = 0; h < 2; h++) { const int q = 2 * (st + 1) + h; if (q - d >= 0) acc[h] = mfma(w2[q - d][e], t2[d - 2][e], acc[h]); }
        *LDSP(f4, fp) = acc[0];
        *LDSP(f4, fp + 1024) = acc[1];
      }
      STAMP(14 + 2 * st);
      bar();
      STAMP(5 + st);
    }
  }
  if (w < 8) store_rows(outp, six, 1536 + 64 * w + lane, dk01, dk23, dk45);
  STAMP(9);
  STAMP_DUMP;
}

extern "C" void kernel_launch(void* const* d_in, const int* in_sizes, int n_in,
                              void* d_out, int out_size, void* d_ws, size_t ws_size,
                              hipStream_t stream) {
  const char* const* in = (const char* const*)d_in;
  Params p;
  p.b[0] = Br{in[0], in[1], in[2], in[3], in[4], in[3], in[4], in[5], in[6]};
  p.b[1] = Br{in[7], in[8], in[9], in[12], in[13], in[10], in[11], in[14], in[15]};
  p.out = (char*)d_out;
  p.dbg = (unsigned long long*)d_ws;
  ssm_fused<<<240, 768, 0, stream>>>(p);
}
```

```cpp
#define AREC 0
#define ASPIN 0
#define PRE 1
#define POST 0
#include <hip/hip_runtime.h>
#include <hip/hip_bf16.h>
#include <stdint.h>

typedef float f2 __attribute__((ext_vector_type(2)));
typedef float f4 __attribute__((ext_vector_type(4)));
typedef __bf16 bf2 __attribute__((ext_vector_type(2)));
typedef __bf16 bf8 __attribute__((ext_vector_type(8)));
typedef unsigned u2 __attribute__((ext_vector_type(2)));
typedef unsigned u3 __attribute__((ext_vector_type(3)));
typedef unsigned u4 __attribute__((ext_vector_type(4)));
typedef unsigned short u16;
#define DI __device__ __forceinline__
#define LDSP(T, off) ((__attribute__((address_space(3))) T*)(unsigned long)(unsigned)(off))
#define CSTP(T, ptr) ((const __attribute__((address_space(4))) T*)(unsigned long)(ptr))
#define SB() __builtin_amdgcn_sched_barrier(0)

#define STAMP(i)
#define STAMP_DECL unsigned long long rt0_; asm volatile("s_memrealtime %0\n\ts_waitcnt lgkmcnt(0)" : "=s"(rt0_) :: "memory"); \
   if (AREC && blockIdx.x == 0 && threadIdx.x == 0) p.dbg[0] = rt0_;
#define STAMP_DUMP do { asm volatile("s_waitcnt vmcnt(0)" ::: "memory"); bar(); if (ASPIN && w == 0) { unsigned long long tgt = p.dbg[0] + 6000ull; \
   for (int it_ = 0; it_ < 200000; it_++) { if (__builtin_amdgcn_s_memrealtime() >= tgt) break; __builtin_amdgcn_s_sleep(1); } } (void)rt0_; } while (0)
__global__ void rec_k(unsigned long long* d) { unsigned long long t = __builtin_amdgcn_s_memrealtime(); if (threadIdx.x == 0) d[0] = t; }
__global__ void spin_k(unsigned long long* d) { unsigned long long tgt = d[0] + 6000ull;
   for (int it_ = 0; it_ < 200000; it_++) { if (__builtin_amdgcn_s_memrealtime() >= tgt) break; __builtin_amdgcn_s_sleep(1); } }

#define CODE_PF_WAVES 3

constexpr int NMODE = 192;
constexpr int SCR = 16 * 288;
constexpr int KNC = 1216;
constexpr int OFF_KN = 0;
constexpr int OFF_KF = 6 * 4 * KNC;
constexpr int KFS = 896;
constexpr int OFF_YS = OFF_KF + 6 * KFS;
constexpr int OFF_RY = 12 * SCR;
constexpr int UT_PAD = 528;
constexpr int UTS = (UT_PAD + 2048) * 2;
constexpr int OFF_UT = OFF_RY + 18 * 3072;
constexpr int LDS_BYTES = OFF_UT + 6 * UTS;
static_assert(OFF_YS + 12288 <= OFF_RY, "lds alias");

#define CM_MUL(d, a, b) "v_pk_mul_f32 " d ", " a ", " b " op_sel_hi:[0,1]\n\t"
#define CM_FMA(d, a, b) "v_pk_fma_f32 " d ", " a ", " b ", " d " op_sel:[1,1,0] op_sel_hi:[1,0,1] neg_lo:[0,1,0]\n\t"
#define CC_MUL(d, a, b) "v_pk_mul_f32 " d ", " a ", " b " op_sel_hi:[0,1] neg_hi:[0,1]\n\t"
#define CC_FMA(d, a, b) "v_pk_fma_f32 " d ", " a ", " b ", " d " op_sel:[1,1,0] op_sel_hi:[1,0,1]\n\t"
DI f2 cmul(f2 a, f2 b) {
  f2 r;
  asm(CM_MUL("%0", "%1", "%2") CM_FMA("%0", "%1", "%2") : "=&v"(r) : "v"(a), "v"(b));
  return r;
}
DI void cmul2(f2& r0, f2& r1, f2 a0, f2 b0, f2 a1, f2 b1) {
  asm(CM_MUL("%0", "%2", "%3") CM_MUL("%1", "%4", "%5") CM_FMA("%0", "%2", "%3") CM_FMA("%1", "%4", "%5")
      : "=&v"(r0), "=&v"(r1) : "v"(a0), "v"(b0), "v"(a1), "v"(b1));
}
DI void cmul3(f2& r0, f2& r1, f2& r2, f2 a, f2 b0, f2 b1, f2 b2) {
  asm(CM_MUL("%0", "%3", "%4") CM_MUL("%1", "%3", "%5") CM_MUL("%2", "%3", "%6")
      CM_FMA("%0", "%3", "%4") CM_FMA("%1", "%3", "%5") CM_FMA("%2", "%3", "%6")
      : "=&v"(r0), "=&v"(r1), "=&v"(r2) : "v"(a), "v"(b0), "v"(b1), "v"(b2));
}
DI void cmul4(f2& r0, f2& r1, f2& r2, f2& r3, f2 a, f2 b0, f2 b1, f2 b2, f2 b3) {
  asm(CM_MUL("%0", "%4", "%5") CM_MUL("%1", "%4", "%6") CM_MUL("%2", "%4", "%7") CM_MUL("%3", "%4", "%8")
      CM_FMA("%0", "%4", "%5") CM_FMA("%1", "%4", "%6") CM_FMA("%2", "%4", "%7") CM_FMA("%3", "%4", "%8")
      : "=&v"(r0), "=&v"(r1), "=&v"(r2), "=&v"(r3) : "v"(a), "v"(b0), "v"(b1), "v"(b2), "v"(b3));
}
DI void cmulc4(f2& r0, f2& r1, f2& r2, f2& r3, f2 a0, f2 a1, f2 a2, f2 a3, f2 b) {
  asm(CC_MUL("%0", "%4", "%8") CC_MUL("%1", "%5", "%8") CC_MUL("%2", "%6", "%8") CC_MUL("%3", "%7", "%8")
      CC_FMA("%0", "%4", "%8") CC_FMA("%1", "%5", "%8") CC_FMA("%2", "%6", "%8") CC_FMA("%3", "%7", "%8")
      : "=&v"(r0), "=&v"(r1), "=&v"(r2), "=&v"(r3) : "v"(a0), "v"(a1), "v"(a2), "v"(a3), "v"(b));
}
DI void cmulc_sq(f2& r0, f2& r1, f2 a, f2 b) {
  asm(CC_MUL("%0", "%2", "%3") CM_MUL("%1", "%3", "%3") CC_FMA("%0", "%2", "%3") CM_FMA("%1", "%3", "%3")
      : "=&v"(r0), "=&v"(r1) : "v"(a), "v"(b));
}
DI void cmulc2_sq(f2& r0, f2& r1, f2& r2, f2 a0, f2 a1, f2 b) {
  asm(CC_MUL("%0", "%3", "%5") CC_MUL("%1", "%4", "%5") CM_MUL("%2", "%5", "%5")
      CC_FMA("%0", "%3", "%5") CC_FMA("%1", "%4", "%5") CM_FMA("%2", "%5", "%5")
      : "=&v"(r0), "=&v"(r1), "=&v"(r2) : "v"(a0), "v"(a1), "v"(b));
}
DI unsigned pk2(float lo, float hi) {
  bf2 r = __builtin_convertvector((f2{lo, hi}), bf2);
  return __builtin_bit_cast(unsigned, r);
}
DI unsigned pk2(f2 a) { return pk2(a.x, a.y); }
DI float bfhi(unsigned x) { return __uint_as_float(x & 0xffff0000u); }
DI float bflo(unsigned x) { return __uint_as_float(x << 16); }
DI float rbf(float f) { return __uint_as_float(pk2(f, 0.f) << 16); }
DI f4 mfma(u4 a, u4 b, f4 c) {
  return __builtin_amdgcn_mfma_f32_16x16x32_bf16(__builtin_bit_cast(bf8, a), __builtin_bit_cast(bf8, b), c, 0, 0, 0);
}
DI void bar() { asm volatile("s_waitcnt lgkmcnt(0)\n\ts_barrier" ::: "memory"); }
template <int R0>
DI void wr4(int m0v, unsigned a, unsigned b, unsigned c, unsigned d) {
  asm volatile("s_mov_b32 m0, %0\n\ts_nop 0\n\t"
               "ds_write_addtid_b32 %1 offset:%5\n\tds_write_addtid_b32 %2 offset:%6\n\t"
               "ds_write_addtid_b32 %3 offset:%7\n\tds_write_addtid_b32 %4 offset:%8"
               :: "s"(m0v), "v"(a), "v"(b), "v"(c), "v"(d),
                  "i"(R0 * 288), "i"((R0 + 1) * 288), "i"((R0 + 2) * 288), "i"((R0 + 3) * 288) : "memory");
}
DI f2 cexp_dir(float a2, float b2) {
  float m = __builtin_amdgcn_exp2f(a2);
  float ph = __builtin_amdgcn_fractf(b2);
  return f2{m * __builtin_amdgcn_cosf(ph), m * __builtin_amdgcn_sinf(ph)};
}
DI float exp_acc(float x) {
  float h = x * 1.44269504088896341f;
  float l = fmaf(x, 1.92596299112661746e-8f, fmaf(x, 1.44269504088896341f, -h));
  float e = __builtin_amdgcn_exp2f(h);
  return fmaf(e * l, 0.69314718055994531f, e);
}
DI float nextlane(float v) {
  return __int_as_float(__builtin_amdgcn_update_dpp(0, __float_as_int(v), 0x130, 0xf, 0xf, true));
}
DI float first(float v) { return __int_as_float(__builtin_amdgcn_readfirstlane(__float_as_int(v))); }
DI int pslot(int g, int a) { return 16 * (16 * g + ((a + 4 * g) & 15)); }

struct Br { const char *u, *are, *aim, *cre, *cim, *bre, *bim, *ldt, *dsk; };
struct Params { Br b[2]; char* out; unsigned long long* dbg; };
struct Raw { unsigned ar, ai, cr, ci, br, bi; };

DI void read4(int ra, u4 (&f)[4]) {
#pragma unroll
  for (int s = 0; s < 4; s++) f[s] = *LDSP(u4, ra + 64 * s);
}

DI void kgen_task(const Raw& r, float ldt, int br, int scr, int ra, int pa) {
  float dt = rbf(exp_acc(ldt));
  float ar, cx, cy;
  if (br == 0) {
    ar = -rbf(exp_acc(bflo(r.ar)));
    float d2 = dt + dt;
    cx = bflo(r.cr) * d2; cy = bflo(r.ci) * d2;
  } else {
    ar = bflo(r.ar);
    float bx = bflo(r.br) * dt, by = bflo(r.bi) * dt, c0 = bflo(r.cr), c1 = bflo(r.ci);
    cx = c0 * bx - c1 * by; cy = c0 * by + c1 * bx;
  }
  float a2 = (ar * dt) * 1.44269504088896341f;
  float b2 = (bflo(r.ai) * dt) * 0.15915494309189535f;
  u4 fa[4], fx[4];
  const f4 zero4 = {0.f, 0.f, 0.f, 0.f};
  f4 d0 = zero4, d1 = zero4, d2 = zero4;
  f2 z1 = cexp_dir(a2, b2);
  f2 z256 = cexp_dir(a2 * 256.f, b2 * 256.f);
  f2 z2, z3, z4, z5, z6, z7, z8, z9, z10, z11, z12, z13, z14, z15, w, w2, w4, z512, z768, z1024, z1280, z1536, z1792, z2048;
  cmul2(z2, z512, z1, z1, z256, z256);
  cmul2(z3, z4, z2, z1, z2, z2);
  wr4<0>(scr, 0x00003f80u, pk2(z1), pk2(z2), pk2(z3));
  cmul4(z8, z5, z6, z7, z4, z4, z1, z2, z3);
  wr4<4>(scr, pk2(z4), pk2(z5), pk2(z6), pk2(z7));
  cmul4(w, z9, z10, z11, z8, z8, z1, z2, z3);
  wr4<8>(scr, pk2(z8), pk2(z9), pk2(z10), pk2(z11));
  f2 x0 = f2{cx, -cy}, x1, x2, x3;
  cmulc_sq(x1, w2, x0, w);
  cmul4(z12, z13, z14, z15, z8, z4, z5, z6, z7);
  wr4<12>(scr, pk2(z12), pk2(z13), pk2(z14), pk2(z15));
  read4(ra, fa);
  cmulc2_sq(x2, x3, w4, x0, x1, w2);
  wr4<0>(scr, pk2(x0), pk2(x1), pk2(x2), pk2(x3));
  f2 y0, y1, y2, y3;
  cmulc4(y0, y1, y2, y3, x0, x1, x2, x3, w4);
  wr4<4>(scr, pk2(y0), pk2(y1), pk2(y2), pk2(y3));
  cmulc4(x0, x1, x2, x3, y0, y1, y2, y3, w4);
  wr4<8>(scr, pk2(x0), pk2(x1), pk2(x2), pk2(x3));
  cmulc4(y0, y1, y2, y3, x0, x1, x2, x3, w4);
  wr4<12>(scr, pk2(y0), pk2(y1), pk2(y2), pk2(y3));
  read4(ra, fx);
  cmul4(z768, y1, y2, y3, z256, z512, z2, z4, z6);
  wr4<0>(scr, pk2(z256), pk2(y1), pk2(y2), pk2(y3));
  cmul4(y0, y1, y2, y3, z256, z8, z10, z12, z14);
  wr4<4>(scr, pk2(y0), pk2(y1), pk2(y2), pk2(y3));
#pragma unroll
  for (int s = 0; s < 4; s++) d0 = mfma(fa[s], fx[s], d0);
  cmul4(z1024, y1, y2, y3, z512, z512, z4, z8, z12);
  wr4<8>(scr, pk2(z512), pk2(y1), pk2(y2), pk2(y3));
  cmul4(z1536, y1, y2, y3, z768, z768, z4, z8, z12);
  wr4<12>(scr, pk2(z768), pk2(y1), pk2(y2), pk2(y3));
  read4(ra, fa);
  cmul4(z1280, z1792, z2048, y0, z1024, z256, z768, z1024, z8);
  cmul3(y1, y2, y3, z8, z1280, z1536, z1792);
  unsigned t0 = pk2(z1024), t1 = pk2(y0), t2 = pk2(z1280), t3 = pk2(y1), t4 = pk2(z1536), t5 = pk2(y2), t6 = pk2(z1792), t7 = pk2(y3), t8 = pk2(z2048);
#pragma unroll
  for (int s = 0; s < 4; s++) d1 = mfma(fa[s], fx[s], d1);
  wr4<0>(scr, t0, t1, t2, t3);
  wr4<4>(scr, t4, t5, t6, t7);
  asm volatile("s_mov_b32 m0, %0\n\ts_nop 0\n\tds_write_addtid_b32 %1 offset:%2" :: "s"(scr), "v"(t8), "i"(8 * 288) : "memory");
  read4(ra, fa);
  *LDSP(f4, pa) = d0;
  *LDSP(f4, pa + 1024) = d1;
#pragma unroll
  for (int s = 0; s < 4; s++) d2 = mfma(fa[s], fx[s], d2);
  *LDSP(f4, pa + 2048) = d2;
}

DI f4 sum3_4(int a) { return *LDSP(f4, a) + *LDSP(f4, a + 3072) + *LDSP(f4, a + 6144); }
DI f2 sum3_2(int a) { return *LDSP(f2, a) + *LDSP(f2, a + 3072) + *LDSP(f2, a + 6144); }
DI float sum3_1(int a) { return *LDSP(float, a) + *LDSP(float, a + 3072) + *LDSP(float, a + 6144); }

DI void put_unit(int a, float v0, float v1, float v2, float v3, float v4, float v5, float v6) {
  unsigned p10 = pk2(v1, v0), p21 = pk2(v2, v1), p32 = pk2(v3, v2), p43 = pk2(v4, v3), p54 = pk2(v5, v4), p65 = pk2(v6, v5);
  *LDSP(u2, a) = u2{p32, p10};
  *LDSP(u2, a + KNC) = u2{p43, p21};
  *LDSP(u2, a + 2 * KNC) = u2{p54, p32};
  *LDSP(u2, a + 3 * KNC) = u2{p65, p43};
}

DI u4 band(int lane, int lo, int n) {
  unsigned m = (((1u << n) - 1u) << lo << (lane & 15)) >> (8 * (lane >> 4));
  u4 r;
#pragma unroll
  for (int j = 0; j < 4; j++)
    r[j] = ((unsigned)__builtin_amdgcn_sbfe(m, 2 * j, 1) & 0xffffu) | ((unsigned)__builtin_amdgcn_sbfe(m, 2 * j + 1, 1) << 16);
  return r;
}
DI u4 band1(int lane, int lo, int n) { u4 r = band(lane, lo, n) & 0x3f803f80u; asm volatile("" : "+v"(r)); return r; }

DI void store_rows(char* outp, bool six, int t, unsigned dk01, unsigned dk23, unsigned dk45) {
  const int ya = OFF_YS + ((t >> 9) & 1) * 6144 + (t & 511) * 2;
  const int ua = OFF_UT + (UT_PAD + t) * 2;
  unsigned y[6], u[6];
#pragma unroll
  for (int c = 0; c < 6; c++) { y[c] = *LDSP(u16, ya + c * 1024); u[c] = *LDSP(u16, ua + c * UTS); }
  unsigned s01 = pk2(bflo(u[0]) * bflo(dk01), bflo(u[1]) * bfhi(dk01));
  unsigned s23 = pk2(bflo(u[2]) * bflo(dk23), bflo(u[3]) * bfhi(dk23));
  unsigned s45 = pk2(bflo(u[4]) * bflo(dk45), bflo(u[5]) * bfhi(dk45));
  unsigned o01 = pk2(bflo(y[0]) + bflo(s01), bflo(y[1]) + bfhi(s01));
  unsigned o23 = pk2(bflo(y[2]) + bflo(s23), bflo(y[3]) + bfhi(s23));
  unsigned o45 = pk2(bflo(y[4]) + bflo(s45), bflo(y[5]) + bfhi(s45));
  char* dst = outp + (unsigned)t * 2560u;
  if (six) *(u3*)dst = u3{o01, o23, o45};
  else *(u2*)dst = u2{o01, o23};
}

template <bool FIRST>
DI void near_stage(const u4 (&ka)[17], const u4& kbr, u4 (&ring)[9], int ub, int fp, int ya) {
  f4 a0 = {0.f, 0.f, 0.f, 0.f}, a1 = a0;
  if (!FIRST) { a0 = *LDSP(f4, fp); a1 = *LDSP(f4, fp + 1024); }
  SB();
#pragma unroll
  for (int n = 0; n < 27; n++) {
    const u4 v = ring[n % 9];
    if (n < 17) a1 = mfma(ka[n], v, a1);
    if (n >= 8 && n < (FIRST ? 17 : 25)) a0 = mfma(ka[n - 8], v, a0);
    if (!FIRST && n == 25) a0 = mfma(kbr, v, a0);
    if (!FIRST && n == 26) a1 = mfma(kbr, v, a1);
    const int m = n + 9;
    if (m < 25) { if (!FIRST || m < 17) ring[n % 9] = *LDSP(u4, ub + 1600 - 64 * m); }
    else if (m < 27) { if (!FIRST) ring[n % 9] = *LDSP(u4, ub + 1600 - 480 - 2064 + 512 * (m - 25)); }
    else ring[n % 9] = *LDSP(u4, ub + 1600 + 1024 - 64 * (m - 27));
    SB();
  }
  *LDSP(u2, ya) = u2{pk2(a0[0], a0[1]), pk2(a0[2], a0[3])};
  *LDSP(u2, ya + 512) = u2{pk2(a1[0], a1[1]), pk2(a1[2], a1[3])};
}

__global__ __launch_bounds__(768, 3) void ssm_fused(Params p) {
  __shared__ __attribute__((aligned(16))) char smem[LDS_BYTES];
  asm volatile("" :: "v"((__attribute__((address_space(3))) char*)smem));
  const int tid = threadIdx.x, lane = tid & 63;
  const int w = __builtin_amdgcn_readfirstlane(tid >> 6);
  const int bx = blockIdx.x & 7, bj = blockIdx.x >> 3;
  const int blk = bx * 5 + bj / 6, sl = bj % 6;
  const bool six = sl < 4;
  const int nch = six ? 6 : 4;
  const int ch0 = blk * 32 + (six ? 6 * sl : 8 + 4 * sl);
  const int br = blk >= 20;
  const int e0 = ch0 - br * 640;
  const Br B = p.b[br];
  STAMP_DECL
  STAMP(0);

  const bool two = six && w < 6;
  const int tk1 = two ? w + 12 : w;
  const unsigned o0 = (e0 * NMODE + w * 64 + lane) * 2u, o1 = (e0 * NMODE + tk1 * 64 + lane) * 2u;
  Raw r0, r1;
  r0.ar = *(const u16*)(B.are + o0); r0.ai = *(const u16*)(B.aim + o0); r0.cr = *(const u16*)(B.cre + o0);
  r0.ci = *(const u16*)(B.cim + o0); r0.br = *(const u16*)(B.bre + o0); r0.bi = *(const u16*)(B.bim + o0);
  unsigned pf0, pf1 = 0, pf2 = 0;
  {
    unsigned row = bj * 69 + tid / 5;
    row = row < 2047u ? row : 2047u;
    unsigned off = row * 1280u + (bx & 3) * 320u + (tid % 5) * 64u;
    if (tid >= 345) off = e0 * 2u;
    asm volatile("global_load_dword %0, %1, %2" : "=v"(pf0) : "v"(off), "s"(B.u) : "memory");
    if (w >= 12 - CODE_PF_WAVES) {
      unsigned long long pc;
      asm volatile("s_getpc_b64 %0" : "=s"(pc));
      unsigned co = ((11 - w) * 64 + lane) * 64u;
      asm volatile("global_load_dword %0, %1, %2" : "=v"(pf1) : "v"(co), "s"(pc & ~63ull) : "memory");
    }
    if (w == 6) {
      unsigned oo = lane * 81920u + ch0 * 2u;
      asm volatile("global_load_dword %0, %1, %2" : "=v"(pf2) : "v"(oo), "s"(p.out) : "memory");
    }
  }
  r1.ar = *(const u16*)(B.are + o1); r1.ai = *(const u16*)(B.aim + o1); r1.cr = *(const u16*)(B.cre + o1);
  r1.ci = *(const u16*)(B.cim + o1); r1.br = *(const u16*)(B.bre + o1); r1.bi = *(const u16*)(B.bim + o1);
  const int c0 = w / 3, c1 = tk1 / 3;
  const unsigned l0 = CSTP(unsigned, B.ldt)[(e0 + c0) >> 1], l1 = CSTP(unsigned, B.ldt)[(e0 + c1) >> 1];
  const unsigned dk01 = CSTP(unsigned, B.dsk)[e0 >> 1], dk23 = CSTP(unsigned, B.dsk)[(e0 >> 1) + 1],
                 dk45 = CSTP(unsigned, B.dsk)[(e0 >> 1) + (six ? 2 : 1)];
  SB();
  if (tid < 6 * 66) *LDSP(u4, OFF_UT + (tid / 66) * UTS + (tid % 66) * 16) = u4{0, 0, 0, 0};
  u4 cst[6];
  if (w >= 6) {
    cst[0] = band1(lane, 16 - 4 - 1, 3);
    cst[1] = band1(lane, 16 - 8 - 2, 5);
    cst[2] = band1(lane, 16 - 12 - 1, 3);
    cst[3] = band1(lane, 24 - 16 - 2, 5);
    cst[4] = band1(lane, 16 - 8 - 3, 7);
    cst[5] = band1(lane, 24 - 16 - 4, 9);
  } else {
    cst[0] = band(lane, 4, 7);
    asm volatile("" : "+v"(cst[0]));
  }
  asm volatile("s_waitcnt vmcnt(0)" : "+v"(pf0), "+v"(pf1), "+v"(pf2) :: "memory");
  STAMP(1);
  u3 ur[4];
  if (w >= 4) {
    const unsigned so = (tid - 256) * 5120u + e0 * 2u - (six ? 0u : 4u);
#pragma unroll
    for (int r = 0; r < 4; r++) ur[r] = *(const u3*)(B.u + (so + r * 1280u));
  }
  {
    const int scr = w * SCR;
    const int ra = scr + (lane & 15) * 288 + (lane >> 4) * 16;
    int pa = OFF_RY + w * 3072 + pslot(lane >> 4, lane & 15);
    float ldt = (c0 & 1) ? bfhi(l0) : bflo(l0);
    for (int it = 0;; it++) {
      kgen_task(r0, ldt, br, scr, ra, pa);
      if (it == 1 || !two) break;
      r0 = r1; ldt = (c1 & 1) ? bfhi(l1) : bflo(l1); pa += 12 * 3072;
    }
  }
  STAMP(2);
  if (w >= 4) {
    const int ua = OFF_UT + (UT_PAD + (tid - 256) * 4) * 2 - (six ? 0 : 2 * UTS);
#pragma unroll
    for (int d = 0; d < 3; d++) {
      if (d > 0 || six) {
        *LDSP(u2, ua + (2 * d) * UTS) = u2{__builtin_amdgcn_perm(ur[1][d], ur[0][d], 0x05040100u), __builtin_amdgcn_perm(ur[3][d], ur[2][d], 0x05040100u)};
        *LDSP(u2, ua + (2 * d + 1) * UTS) = u2{__builtin_amdgcn_perm(ur[1][d], ur[0][d], 0x07060302u), __builtin_amdgcn_perm(ur[3][d], ur[2][d], 0x07060302u)};
      }
    }
  }
  const int c = w < 6 ? w : w - 6;
  const int la = lane & 15, lg = lane >> 4;
  const int part = OFF_RY + c * 3 * 3072;
  const int ubase = OFF_UT + c * UTS + UT_PAD * 2 + la * 32 + lg * 16;
  char* outp = p.out + ch0 * 2;
  const f4 zero4 = {0.f, 0.f, 0.f, 0.f};
  bar();
  STAMP(3);

  if (w < 6) {
    if (c < nch) {
      u4 ka[17], kbr, ring[9];
      const int kn = OFF_KN + c * 4 * KNC;
      f4 va = sum3_4(part + pslot(lane & 3, lane >> 2));
      f2 vb = sum3_2(part + 1024 + pslot((lane >> 1) & 1, lane >> 2) + (lane & 1) * 8);
      float k512 = sum3_1(part + 1024 + pslot(2, 0));
      float k1024 = sum3_1(part + 2048 + pslot(0, 0));
      int ub = ubase + 480 - 1600;
#pragma unroll
      for (int n = 0; n < 9; n++) ring[n] = *LDSP(u4, ub + 1600 - 64 * n);
      float k256 = first(vb.x), k258 = first(vb.y), k0 = first(va.x), k1 = first(va.y), k2 = first(va.z);
      float n0 = nextlane(va.x), n1 = nextlane(va.y), n2 = nextlane(va.z);
      float ne = nextlane(vb.x), no = nextlane(vb.y);
      if (lane == 63) { n0 = k256; n1 = k256; n2 = k258; ne = k512; no = k512; }
      STAMP(10);
      put_unit(kn + (135 - lane) * 8, va.x, va.y, va.z, va.w, n0, n1, n2);
      put_unit(kn + (71 - lane) * 8, vb.x, vb.x, vb.y, ne, ne, ne, no);
      if (lane < 13) {
        int i = lane < 5 ? lane - 5 : 123 + lane;
        float a = (i == 128) ? k512 : 0.f;
        bool m1 = (i == -1);
        put_unit(kn + (135 - i) * 8, a, a, a, 0.f, m1 ? k0 : 0.f, m1 ? k1 : 0.f, m1 ? k2 : 0.f);
      }
      STAMP(11);
      const int s = (la + 1) & 3;
      const int kr = kn + s * KNC + (543 - (la - 8 * lg + 16) + s) * 2 - 1024;
#pragma unroll
      for (int m = 0; m < 17; m++) {
        u2 lo = *LDSP(u2, kr + 1024 - 64 * m), hi = *LDSP(u2, kr + 1024 - 64 * m + 8);
        ka[m] = u4{lo.x, lo.y, hi.x, hi.y};
      }
      kbr = cst[0] & pk2(k1024, k1024);
      int fp = OFF_RY + c * 2048 + lane * 16;
      int ya = OFF_YS + c * 1024 + la * 32 + lg * 8;
      STAMP(12);
      bar();
      STAMP(4);
      near_stage<true>(ka, kbr, ring, ub, fp, ya);
      STAMP(13);
      bar();
      STAMP(5);
      for (int st = 1; st < 4; st++) {
        ub += 1024;
        near_stage<false>(ka, kbr, ring, ub, OFF_RY + (st & 1) * 12288 + c * 2048 + lane * 16, OFF_YS + (st & 1) * 6144 + c * 1024 + la * 32 + lg * 8);
        if (st == 1) STAMP(14);
        bar();
        if (st == 1) STAMP(6); if (st == 2) STAMP(7); if (st == 3) STAMP(8);
      }
    } else {
      for (int i = 0; i < 5; i++) bar();
    }
  } else {
    u4 t2[3][2], t3[4];
    const bool act = c < nch;
    u4 ua0, uc0, ua1, uc1;
    if (act) {
      const int kf = OFF_KF + c * KFS;
      f2 v3 = sum3_2(part + 2048 + pslot(lane >> 5, lane & 15) + ((lane >> 4) & 1) * 8);
      f4 v2 = sum3_4(part + 1024 + pslot(2 + ((lane >> 4) & 1), lane & 15));
      float k2048 = sum3_1(part + 2048 + pslot(2, 0));
      ua0 = *LDSP(u4, ubase - 32); uc0 = *LDSP(u4, ubase - 48); ua1 = *LDSP(u4, ubase + 512 - 32); uc1 = *LDSP(u4, ubase + 512 - 48);
      float n3 = nextlane(v3.x), n2 = nextlane(v2.x);
      if (lane == 63) n3 = k2048;
      if (lane == 31) n2 = 0.f;
      *LDSP(unsigned, kf + 512 + (63 - lane) * 4) = pk2(v3.y, n3);
      if (lane < 16) *LDSP(unsigned, kf + 512 + (64 + lane) * 4) = 0u;
      {
        int x = lane < 32 ? 47 - lane : (lane < 48 ? lane - 32 : lane);
        bool nz = lane < 32;
        *LDSP(unsigned, kf + x * 4) = nz ? pk2(v2.y, v2.z) : 0u;
        *LDSP(unsigned, kf + 256 + x * 4) = nz ? pk2(v2.w, n2) : 0u;
      }
      int a = kf + (15 - la + 4 * lg) * 4;
      asm volatile("" : "+v"(a));
#pragma unroll
      for (int d = 0; d < 3; d++)
#pragma unroll
        for (int h = 0; h < 2; h++)
#pragma unroll
          for (int j = 0; j < 4; j++) t2[d][h][j] = *LDSP(unsigned, a + h * 256 + (32 - 16 * d + j) * 4);
#pragma unroll
      for (int d = 0; d < 4; d++)
#pragma unroll
        for (int j = 0; j < 4; j++) t3[d][j] = *LDSP(unsigned, a + 512 + (48 - 16 * d + j) * 4);
    }
    STAMP(12);
    bar();
    STAMP(4);
    u4 w2[6][2], w3[4];
#pragma unroll
    for (int st = 0; st < 4; st++) {
      if (st > 0) {
        for (int rw = c; rw < 8; rw += 6) store_rows(outp, six, 512 * (st - 1) + 64 * rw + lane, dk01, dk23, dk45);
      }
      STAMP(13 + 2 * st);
      if (st < 3 && act) {
#pragma unroll
        for (int h = 0; h < 2; h++) {
          const int be = 2 * st + h;
          const u4 ua = h ? ua1 : ua0, uc = h ? uc1 : uc0;
          f4 q0 = mfma(ua, cst[0], zero4), q1 = mfma(ua, cst[1], zero4), q2 = mfma(ua, cst[2], zero4), q3 = mfma(uc, cst[3], zero4);
          f4 q4, q5;
          if (be < 4) { q4 = mfma(ua, cst[4], zero4); q5 = mfma(uc, cst[5], zero4); }
#pragma unroll
          for (int j = 0; j < 4; j++) { w2[be][0][j] = pk2(q0[j], q1[j]); w2[be][1][j] = pk2(q2[j], q3[j]); }
          if (be < 4) {
#pragma unroll
            for (int j = 0; j < 4; j++) w3[be][j] = pk2(q4[j], q5[j]);
          }
        }
        if (st < 2) {
          const int nb = ubase + 1024 * (st + 1);
          ua0 = *LDSP(u4, nb - 32); uc0 = *LDSP(u4, nb - 48); ua1 = *LDSP(u4, nb + 512 - 32); uc1 = *LDSP(u4, nb + 512 - 48);
        }
        const int fp = OFF_RY + ((st + 1) & 1) * 12288 + c * 2048 + lane * 16;
        f4 acc[2] = {zero4, zero4};
#pragma unroll
        for (int d = 7; d >= 4; d--)
#pragma unroll
          for (int h = 0; h < 2; h++) { const int q = 2 * (st + 1) + h; if (q - d >= 0) acc[h] = mfma(w3[q - d], t3[d - 4], acc[h]); }
#pragma unroll
        for (int d = 4; d >= 2; d--)
#pragma unroll
          for (int e = 0; e < 2; e++)
#pragma unroll
            for (int h = 0; h < 2; h++) { const int q = 2 * (st + 1) + h; if (q - d >= 0) acc[h] = mfma(w2[q - d][e], t2[d - 2][e], acc[h]); }
        *LDSP(f4, fp) = acc[0];
        *LDSP(f4, fp + 1024) = acc[1];
      }
      STAMP(14 + 2 * st);
      bar();
      STAMP(5 + st);
    }
  }
  if (w < 8) store_rows(outp, six, 1536 + 64 * w + lane, dk01, dk23, dk45);
  STAMP(9);
  STAMP_DUMP;
}

extern "C" void kernel_launch(void* const* d_in, const int* in_sizes, int n_in,
                              void* d_out, int out_size, void* d_ws, size_t ws_size,
                              hipStream_t stream) {
  const char* const* in = (const char* const*)d_in;
  Params p;
  p.b[0] = Br{in[0], in[1], in[2], in[3], in[4], in[3], in[4], in[5], in[6]};
  p.b[1] = Br{in[7], in[8], in[9], in[12], in[13], in[10], in[11], in[14], in[15]};
  p.out = (char*)d_out;
  p.dbg = (unsigned long long*)d_ws;
  if (PRE) rec_k<<<1, 64, 0, stream>>>(p.dbg);
  ssm_fused<<<240, 768, 0, stream>>>(p);
  if (POST) spin_k<<<1, 64, 0, stream>>>(p.dbg);
}
```

```cpp
#include <hip/hip_runtime.h>
#include <hip/hip_bf16.h>
#include <stdint.h>


typedef float f2 __attribute__((ext_vector_type(2)));
typedef __bf16 bf2 __attribute__((ext_vector_type(2)));
typedef unsigned u2 __attribute__((ext_vector_type(2)));
typedef unsigned u3 __attribute__((ext_vector_type(3)));
typedef unsigned u4 __attribute__((ext_vector_type(4)));
typedef unsigned short u16;
#define DI __device__ __forceinline__
#define LDSP(T, off) ((__attribute__((address_space(3))) T*)(unsigned long)(unsigned)(off))
#define CSTP(T, ptr) ((const __attribute__((address_space(4))) T*)(unsigned long)(ptr))
#define SB() __builtin_amdgcn_sched_barrier(0)

constexpr int NMODE = 192;
constexpr int ROWB = 288;
constexpr int SCR = 16 * ROWB;
constexpr int KNC = 1216;
constexpr int OFF_PK = 15 * SCR;
constexpr int PKS = 4 * KNC + 3072 + 1632;
constexpr int UT_PAD = 288;
constexpr int UTS = (UT_PAD + 2048) * 2;
constexpr int OFF_UT = OFF_PK + 5 * PKS;
constexpr int OFF_BAND = OFF_UT + 5 * UTS;
constexpr int OFF_FLAG = OFF_BAND + 2048;
constexpr int OFF_G63 = OFF_FLAG + 128;
constexpr int LDS_BYTES = OFF_G63 + 80;
static_assert(LDS_BYTES <= 163840 && PKS % 16 == 0, "lds map");

DI unsigned pk2(float lo, float hi) {
  bf2 r = __builtin_convertvector((f2{lo, hi}), bf2);
  return __builtin_bit_cast(unsigned, r);
}
DI unsigned pk2(f2 a) { return pk2(a.x, a.y); }
DI float bfhi(unsigned x) { return __uint_as_float(x & 0xffff0000u); }
DI float bflo(unsigned x) { return __uint_as_float(x << 16); }
DI float rbf(float f) { return __uint_as_float(pk2(f, 0.f) << 16); }
DI void set_m0(int v) { asm volatile("s_mov_b32 m0, %0\n\ts_nop 0" :: "s"(v) : "memory"); }
DI float exp_acc(float x) {
  float h = x * 1.44269504088896341f;
  float l = fmaf(x, 1.92596299112661746e-8f, fmaf(x, 1.44269504088896341f, -h));
  float e = __builtin_amdgcn_exp2f(h);
  return fmaf(e * l, 0.69314718055994531f, e);
}
DI int pslot(int g, int a) { return 16 * (16 * g + ((a + 4 * g) & 15)); }

struct Br { const char *u, *are, *aim, *cre, *cim, *bre, *bim, *ldt, *dsk; };
struct Params { Br b[2]; char* out; };
struct Raw { float ar, ai, cr, ci, br, bi; };


DI void kgen_tables(float a2, float b2, float cx, float cy, int wa, int ra, int pa, int pb, int fc, int w) {
  unsigned s0, s1, s2, s3, s4;
  asm volatile(
      "v_cmp_lt_f32 vcc, 0, %[a2]\n\t"
      "s_cbranch_vccnz 70f\n\t"
      "v_exp_f32 v68, %[a2]\n\t"
      "v_fract_f32 v69, %[b2]\n\t"
      "v_max_f32 v70, |%[cx]|, |%[cy]|\n\t"
      "v_cos_f32 v71, v69\n\t"
      "v_sin_f32 v72, v69\n\t"
      "v_mov_b32 v69, 0x3c00\n\t"
      "v_max_u32_dpp v70, v70, v70 row_shr:1 row_mask:0xf bank_mask:0xf bound_ctrl:0\n\t"
      "ds_write_addtid_b32 v69 offset:0\n\t"
      "v_mul_f32 v74, v68, v71\n\t"
      "v_mul_f32 v75, v68, v72\n\t"
      "v_max_u32_dpp v70, v70, v70 row_shr:2 row_mask:0xf bank_mask:0xf bound_ctrl:0\n\t"
      "v_pk_mul_f32 v[68:69], v[74:75], v[74:75] op_sel_hi:[0,1]\n\t"
      "v_pk_fma_f32 v[68:69], v[74:75], v[74:75], v[68:69] op_sel:[1,1,0] op_sel_hi:[1,0,1] neg_lo:[0,1,0]\n\t"
      "v_cvt_pk_f16_f32 v71, v74, v75\n\t"
      "v_max_u32_dpp v70, v70, v70 row_shr:4 row_mask:0xf bank_mask:0xf bound_ctrl:0\n\t"
      "ds_write_addtid_b32 v71 offset:288\n\t"
      "v_pk_mul_f32 v[72:73], v[68:69], v[68:69] op_sel_hi:[0,1]\n\t"
      "v_pk_fma_f32 v[72:73], v[68:69], v[68:69], v[72:73] op_sel:[1,1,0] op_sel_hi:[1,0,1] neg_lo:[0,1,0]\n\t"
      "v_cvt_pk_f16_f32 v74, v68, v69\n\t"
      "v_max_u32_dpp v70, v70, v70 row_shr:8 row_mask:0xf bank_mask:0xf bound_ctrl:0\n\t"
      "ds_write_addtid_b32 v74 offset:576\n\t"
      "v_pk_mul_f32 v[68:69], v[72:73], v[72:73] op_sel_hi:[0,1]\n\t"
      "v_pk_mul_f16 v75, v74, v71 op_sel_hi:[0,1]\n\t"
      "v_pk_fma_f32 v[68:69], v[72:73], v[72:73], v[68:69] op_sel:[1,1,0] op_sel_hi:[1,0,1] neg_lo:[0,1,0]\n\t"
      "v_pk_fma_f16 v75, v74, v71, v75 op_sel:[1,1,0] op_sel_hi:[1,0,1] neg_lo:[0,1,0]\n\t"
      "v_cvt_pk_f16_f32 v76, v72, v73\n\t"
      "ds_write_addtid_b32 v75 offset:864\n\t"
      "v_max_u32_dpp v70, v70, v70 row_bcast:15 row_mask:0xa bank_mask:0xf\n\t"
      "v_pk_mul_f32 v[72:73], v[68:69], v[68:69] op_sel_hi:[0,1]\n\t"
      "ds_write_addtid_b32 v76 offset:1152\n\t"
      "v_pk_mul_f16 v77, v76, v71 op_sel_hi:[0,1]\n\t"
      "v_pk_mul_f16 v78, v76, v74 op_sel_hi:[0,1]\n\t"
      "v_pk_mul_f16 v79, v76, v75 op_sel_hi:[0,1]\n\t"
      "v_pk_fma_f32 v[72:73], v[68:69], v[68:69], v[72:73] op_sel:[1,1,0] op_sel_hi:[1,0,1] neg_lo:[0,1,0]\n\t"
      "v_pk_fma_f16 v77, v76, v71, v77 op_sel:[1,1,0] op_sel_hi:[1,0,1] neg_lo:[0,1,0]\n\t"
      "v_pk_fma_f16 v78, v76, v74, v78 op_sel:[1,1,0] op_sel_hi:[1,0,1] neg_lo:[0,1,0]\n\t"
      "ds_write_addtid_b32 v77 offset:1440\n\t"
      "v_pk_fma_f16 v79, v76, v75, v79 op_sel:[1,1,0] op_sel_hi:[1,0,1] neg_lo:[0,1,0]\n\t"
      "v_max_u32_dpp v70, v70, v70 row_bcast:31 row_mask:0xc bank_mask:0xf\n\t"
      "ds_write_addtid_b32 v78 offset:1728\n\t"
      "v_cvt_pk_f16_f32 v80, v68, v69\n\t"
      "v_pk_mul_f32 v[68:69], v[72:73], v[72:73] op_sel_hi:[0,1]\n\t"
      "ds_write_addtid_b32 v79 offset:2016\n\t"
      "v_pk_mul_f16 v81, v80, v76 op_sel_hi:[0,1]\n\t"
      "v_pk_fma_f32 v[68:69], v[72:73], v[72:73], v[68:69] op_sel:[1,1,0] op_sel_hi:[1,0,1] neg_lo:[0,1,0]\n\t"
      "ds_write_addtid_b32 v80 offset:2304\n\t"
      "v_pk_fma_f16 v81, v80, v76, v81 op_sel:[1,1,0] op_sel_hi:[1,0,1] neg_lo:[0,1,0]\n\t"
      "v_readlane_b32 %[s0], v70, 63\n\t"
      "v_pk_mul_f16 v70, v80, v71 op_sel_hi:[0,1]\n\t"
      "v_pk_mul_f16 v82, v80, v74 op_sel_hi:[0,1]\n\t"
      "v_pk_mul_f16 v83, v80, v75 op_sel_hi:[0,1]\n\t"
      "v_pk_mul_f32 v[84:85], v[68:69], v[68:69] op_sel_hi:[0,1]\n\t"
      "v_pk_fma_f16 v70, v80, v71, v70 op_sel:[1,1,0] op_sel_hi:[1,0,1] neg_lo:[0,1,0]\n\t"
      "v_pk_fma_f16 v82, v80, v74, v82 op_sel:[1,1,0] op_sel_hi:[1,0,1] neg_lo:[0,1,0]\n\t"
      "ds_write_addtid_b32 v70 offset:2592\n\t"
      "v_pk_fma_f16 v83, v80, v75, v83 op_sel:[1,1,0] op_sel_hi:[1,0,1] neg_lo:[0,1,0]\n\t"
      "v_pk_fma_f32 v[84:85], v[68:69], v[68:69], v[84:85] op_sel:[1,1,0] op_sel_hi:[1,0,1] neg_lo:[0,1,0]\n\t"
      "ds_write_addtid_b32 v82 offset:2880\n\t"
      "s_lshr_b32 %[s0], %[s0], 23\n\t"
      "s_max_u32 %[s0], %[s0], 14\n\t"
      "ds_write_addtid_b32 v83 offset:3168\n\t"
      "s_lshl_b32 %[s0], %[s0], 23\n\t"
      "s_sub_u32 %[s1], 0x85800000, %[s0]\n\t"
      "ds_write_addtid_b32 v81 offset:3456\n\t"
      "s_sub_u32 %[s0], %[s0], 0x06800000\n\t"
      "s_cmp_lt_u32 %[w], 5\n\t"
      "v_pk_mul_f16 v75, v80, v77 op_sel_hi:[0,1]\n\t"
      "v_pk_mul_f16 v83, v80, v78 op_sel_hi:[0,1]\n\t"
      "v_pk_mul_f16 v70, v80, v79 op_sel_hi:[0,1]\n\t"
      "v_pk_fma_f16 v75, v80, v77, v75 op_sel:[1,1,0] op_sel_hi:[1,0,1] neg_lo:[0,1,0]\n\t"
      "v_pk_fma_f16 v83, v80, v78, v83 op_sel:[1,1,0] op_sel_hi:[1,0,1] neg_lo:[0,1,0]\n\t"
      "ds_write_addtid_b32 v75 offset:3744\n\t"
      "v_pk_fma_f16 v70, v80, v79, v70 op_sel:[1,1,0] op_sel_hi:[1,0,1] neg_lo:[0,1,0]\n\t"
      "ds_write_addtid_b32 v83 offset:4032\n\t"
      "v_mul_f32 v86, %[s1], %[cx]\n\t"
      "ds_write_addtid_b32 v70 offset:4320\n\t"
      "v_mul_f32_e64 v87, %[s1], -%[cy]\n\t"
      "ds_read_b128 v[88:91], %[ra] offset:0\n\t"
      "v_cvt_pk_f16_f32 v75, v72, v73\n\t"
      "v_cvt_pk_f16_f32 v77, v68, v69\n\t"
      "ds_read_b128 v[68:71], %[ra] offset:64\n\t"
      "v_pk_mul_f32 v[72:73], v[86:87], v[84:85] op_sel_hi:[0,1] neg_hi:[0,1]\n\t"
      "v_pk_mul_f16 v79, v77, v75 op_sel_hi:[0,1]\n\t"
      "ds_read_b128 v[92:95], %[ra] offset:128\n\t"
      "v_pk_fma_f32 v[72:73], v[86:87], v[84:85], v[72:73] op_sel:[1,1,0] op_sel_hi:[1,0,1]\n\t"
      "v_pk_fma_f16 v79, v77, v75, v79 op_sel:[1,1,0] op_sel_hi:[1,0,1] neg_lo:[0,1,0]\n\t"
      "ds_read_b128 v[96:99], %[ra] offset:192\n\t"
      "v_cvt_pk_f16_f32 v100, v86, v87\n\t"
      "v_pk_mul_f16 v101, v100, v75 op_sel_hi:[0,1] neg_hi:[0,1]\n\t"
      "ds_write_addtid_b32 v100 offset:0\n\t"
      "v_pk_mul_f16 v86, v100, v77 op_sel_hi:[0,1] neg_hi:[0,1]\n\t"
      "v_pk_mul_f16 v87, v100, v79 op_sel_hi:[0,1] neg_hi:[0,1]\n\t"
      "v_pk_mul_f32 v[102:103], v[72:73], v[84:85] op_sel_hi:[0,1] neg_hi:[0,1]\n\t"
      "v_pk_fma_f16 v101, v100, v75, v101 op_sel:[1,1,0] op_sel_hi:[1,0,1]\n\t"
      "v_pk_fma_f16 v86, v100, v77, v86 op_sel:[1,1,0] op_sel_hi:[1,0,1]\n\t"
      "ds_write_addtid_b32 v101 offset:288\n\t"
      "v_pk_fma_f16 v87, v100, v79, v87 op_sel:[1,1,0] op_sel_hi:[1,0,1]\n\t"
      "v_pk_fma_f32 v[102:103], v[72:73], v[84:85], v[102:103] op_sel:[1,1,0] op_sel_hi:[1,0,1]\n\t"
      "ds_write_addtid_b32 v86 offset:576\n\t"
      "v_cvt_pk_f16_f32 v86, v72, v73\n\t"
      "v_pk_mul_f16 v72, v86, v75 op_sel_hi:[0,1] neg_hi:[0,1]\n\t"
      "ds_write_addtid_b32 v87 offset:864\n\t"
      "v_pk_mul_f16 v73, v86, v77 op_sel_hi:[0,1] neg_hi:[0,1]\n\t"
      "v_pk_mul_f16 v87, v86, v79 op_sel_hi:[0,1] neg_hi:[0,1]\n\t"
      "ds_write_addtid_b32 v86 offset:1152\n\t"
      "v_pk_mul_f32 v[100:101], v[102:103], v[84:85] op_sel_hi:[0,1] neg_hi:[0,1]\n\t"
      "v_pk_fma_f16 v72, v86, v75, v72 op_sel:[1,1,0] op_sel_hi:[1,0,1]\n\t"
      "v_pk_fma_f16 v73, v86, v77, v73 op_sel:[1,1,0] op_sel_hi:[1,0,1]\n\t"
      "ds_write_addtid_b32 v72 offset:1440\n\t"
      "v_pk_fma_f16 v87, v86, v79, v87 op_sel:[1,1,0] op_sel_hi:[1,0,1]\n\t"
      "v_pk_fma_f32 v[100:101], v[102:103], v[84:85], v[100:101] op_sel:[1,1,0] op_sel_hi:[1,0,1]\n\t"
      "ds_write_addtid_b32 v73 offset:1728\n\t"
      "v_cvt_pk_f16_f32 v86, v102, v103\n\t"
      "v_pk_mul_f16 v72, v86, v75 op_sel_hi:[0,1] neg_hi:[0,1]\n\t"
      "ds_write_addtid_b32 v87 offset:2016\n\t"
      "v_pk_mul_f16 v73, v86, v77 op_sel_hi:[0,1] neg_hi:[0,1]\n\t"
      "v_pk_mul_f16 v87, v86, v79 op_sel_hi:[0,1] neg_hi:[0,1]\n\t"
      "ds_write_addtid_b32 v86 offset:2304\n\t"
      "v_pk_mul_f32 v[102:103], v[84:85], v[84:85] op_sel_hi:[0,1]\n\t"
      "v_pk_fma_f16 v72, v86, v75, v72 op_sel:[1,1,0] op_sel_hi:[1,0,1]\n\t"
      "v_pk_fma_f16 v73, v86, v77, v73 op_sel:[1,1,0] op_sel_hi:[1,0,1]\n\t"
      "ds_write_addtid_b32 v72 offset:2592\n\t"
      "v_pk_fma_f16 v87, v86, v79, v87 op_sel:[1,1,0] op_sel_hi:[1,0,1]\n\t"
      "v_pk_fma_f32 v[102:103], v[84:85], v[84:85], v[102:103] op_sel:[1,1,0] op_sel_hi:[1,0,1] neg_lo:[0,1,0]\n\t"
      "ds_write_addtid_b32 v73 offset:2880\n\t"
      "v_cvt_pk_f16_f32 v86, v100, v101\n\t"
      "v_pk_mul_f16 v72, v86, v75 op_sel_hi:[0,1] neg_hi:[0,1]\n\t"
      "ds_write_addtid_b32 v87 offset:3168\n\t"
      "v_pk_mul_f16 v73, v86, v77 op_sel_hi:[0,1] neg_hi:[0,1]\n\t"
      "v_pk_mul_f16 v87, v86, v79 op_sel_hi:[0,1] neg_hi:[0,1]\n\t"
      "ds_write_addtid_b32 v86 offset:3456\n\t"
      "v_pk_mul_f32 v[84:85], v[102:103], v[102:103] op_sel_hi:[0,1]\n\t"
      "v_pk_fma_f16 v72, v86, v75, v72 op_sel:[1,1,0] op_sel_hi:[1,0,1]\n\t"
      "v_pk_fma_f16 v73, v86, v77, v73 op_sel:[1,1,0] op_sel_hi:[1,0,1]\n\t"
      "ds_write_addtid_b32 v72 offset:3744\n\t"
      "v_pk_fma_f16 v87, v86, v79, v87 op_sel:[1,1,0] op_sel_hi:[1,0,1]\n\t"
      "ds_write_addtid_b32 v73 offset:4032\n\t"
      "v_pk_fma_f32 v[84:85], v[102:103], v[102:103], v[84:85] op_sel:[1,1,0] op_sel_hi:[1,0,1] neg_lo:[0,1,0]\n\t"
      "ds_write_addtid_b32 v87 offset:4320\n\t"
      "v_cvt_pk_f16_f32 v75, v84, v85\n\t"
      "ds_read_b128 v[100:103], %[ra] offset:0\n\t"
      "v_pk_mul_f32 v[72:73], v[84:85], v[84:85] op_sel_hi:[0,1]\n\t"
      "ds_read_b128 v[104:107], %[ra] offset:64\n\t"
      "v_pk_fma_f32 v[72:73], v[84:85], v[84:85], v[72:73] op_sel:[1,1,0] op_sel_hi:[1,0,1] neg_lo:[0,1,0]\n\t"
      "v_pk_mul_f16 v77, v75, v74 op_sel_hi:[0,1]\n\t"
      "ds_read_b128 v[84:87], %[ra] offset:128\n\t"
      "v_pk_mul_f16 v79, v75, v76 op_sel_hi:[0,1]\n\t"
      "v_pk_mul_f16 v108, v75, v78 op_sel_hi:[0,1]\n\t"
      "ds_read_b128 v[110:113], %[ra] offset:192\n\t"
      "v_pk_mul_f32 v[114:115], v[72:73], v[72:73] op_sel_hi:[0,1]\n\t"
      "v_pk_fma_f16 v77, v75, v74, v77 op_sel:[1,1,0] op_sel_hi:[1,0,1] neg_lo:[0,1,0]\n\t"
      "ds_write_addtid_b32 v75 offset:0\n\t"
      "v_pk_fma_f16 v79, v75, v76, v79 op_sel:[1,1,0] op_sel_hi:[1,0,1] neg_lo:[0,1,0]\n\t"
      "s_waitcnt lgkmcnt(4)\n\t"
      "v_mfma_f32_16x16x32_f16 v[116:119], v[88:91], v[100:103], 0\n\t"
      "s_waitcnt lgkmcnt(3)\n\t"
      "v_mfma_f32_16x16x32_f16 v[116:119], v[68:71], v[104:107], v[116:119]\n\t"
      "s_waitcnt lgkmcnt(2)\n\t"
      "v_mfma_f32_16x16x32_f16 v[116:119], v[92:95], v[84:87], v[116:119]\n\t"
      "s_waitcnt lgkmcnt(1)\n\t"
      "v_mfma_f32_16x16x32_f16 v[116:119], v[96:99], v[110:113], v[116:119]\n\t"
      "v_pk_fma_f16 v108, v75, v78, v108 op_sel:[1,1,0] op_sel_hi:[1,0,1] neg_lo:[0,1,0]\n\t"
      "ds_write_addtid_b32 v77 offset:288\n\t"
      "v_pk_fma_f32 v[114:115], v[72:73], v[72:73], v[114:115] op_sel:[1,1,0] op_sel_hi:[1,0,1] neg_lo:[0,1,0]\n\t"
      "v_cvt_pk_f16_f32 v74, v72, v73\n\t"
      "ds_write_addtid_b32 v79 offset:576\n\t"
      "v_pk_mul_f16 v77, v75, v80 op_sel_hi:[0,1]\n\t"
      "v_pk_mul_f16 v109, v75, v82 op_sel_hi:[0,1]\n\t"
      "ds_write_addtid_b32 v108 offset:864\n\t"
      "v_mul_f32 v116, %[s0], v116\n\t"
      "v_mul_f32 v117, %[s0], v117\n\t"
      "v_mul_f32 v118, %[s0], v118\n\t"
      "v_mul_f32 v119, %[s0], v119\n\t"
      "ds_write_b128 %[pa], v[116:119] offset:0\n\t"
      "ds_add_u32 %[fc], %[one] offset:96\n\t"
      "s_cbranch_scc1 1f\n\ts_setprio 2\n\t1:\n\t"
      "v_pk_mul_f16 v108, v75, v81 op_sel_hi:[0,1]\n\t"
      "v_pk_mul_f16 v72, v75, v83 op_sel_hi:[0,1]\n\t"
      "v_pk_fma_f16 v77, v75, v80, v77 op_sel:[1,1,0] op_sel_hi:[1,0,1] neg_lo:[0,1,0]\n\t"
      "v_pk_fma_f16 v109, v75, v82, v109 op_sel:[1,1,0] op_sel_hi:[1,0,1] neg_lo:[0,1,0]\n\t"
      "ds_write_addtid_b32 v77 offset:1152\n\t"
      "v_pk_fma_f16 v108, v75, v81, v108 op_sel:[1,1,0] op_sel_hi:[1,0,1] neg_lo:[0,1,0]\n\t"
      "v_pk_fma_f16 v72, v75, v83, v72 op_sel:[1,1,0] op_sel_hi:[1,0,1] neg_lo:[0,1,0]\n\t"
      "ds_write_addtid_b32 v109 offset:1440\n\t"
      "v_cvt_pk_f16_f32 v73, v114, v115\n\t"
      "v_pk_mul_f16 v77, v75, v74 op_sel_hi:[0,1]\n\t"
      "ds_write_addtid_b32 v108 offset:1728\n\t"
      "v_pk_mul_f16 v78, v74, v76 op_sel_hi:[0,1]\n\t"
      "v_pk_mul_f16 v79, v74, v80 op_sel_hi:[0,1]\n\t"
      "ds_write_addtid_b32 v72 offset:2016\n\t"
      "v_pk_mul_f16 v72, v74, v81 op_sel_hi:[0,1]\n\t"
      "v_pk_fma_f16 v77, v75, v74, v77 op_sel:[1,1,0] op_sel_hi:[1,0,1] neg_lo:[0,1,0]\n\t"
      "ds_write_addtid_b32 v74 offset:2304\n\t"
      "v_pk_fma_f16 v78, v74, v76, v78 op_sel:[1,1,0] op_sel_hi:[1,0,1] neg_lo:[0,1,0]\n\t"
      "v_pk_fma_f16 v79, v74, v80, v79 op_sel:[1,1,0] op_sel_hi:[1,0,1] neg_lo:[0,1,0]\n\t"
      "ds_write_addtid_b32 v78 offset:2592\n\t"
      "v_pk_fma_f16 v72, v74, v81, v72 op_sel:[1,1,0] op_sel_hi:[1,0,1] neg_lo:[0,1,0]\n\t"
      "v_pk_mul_f16 v78, v77, v76 op_sel_hi:[0,1]\n\t"
      "ds_write_addtid_b32 v79 offset:2880\n\t"
      "v_pk_mul_f16 v79, v77, v80 op_sel_hi:[0,1]\n\t"
      "v_pk_mul_f16 v82, v77, v81 op_sel_hi:[0,1]\n\t"
      "ds_write_addtid_b32 v72 offset:3168\n\t"
      "v_pk_fma_f16 v78, v77, v76, v78 op_sel:[1,1,0] op_sel_hi:[1,0,1] neg_lo:[0,1,0]\n\t"
      "v_pk_fma_f16 v79, v77, v80, v79 op_sel:[1,1,0] op_sel_hi:[1,0,1] neg_lo:[0,1,0]\n\t"
      "ds_write_addtid_b32 v77 offset:3456\n\t"
      "v_pk_fma_f16 v82, v77, v81, v82 op_sel:[1,1,0] op_sel_hi:[1,0,1] neg_lo:[0,1,0]\n\t"
      "ds_write_addtid_b32 v78 offset:3744\n\t"
      "v_pk_mul_f16 v72, v73, v75 op_sel_hi:[0,1]\n\t"
      "ds_write_addtid_b32 v79 offset:4032\n\t"
      "v_pk_mul_f16 v76, v73, v74 op_sel_hi:[0,1]\n\t"
      "ds_write_addtid_b32 v82 offset:4320\n\t"
      "v_pk_mul_f16 v81, v73, v77 op_sel_hi:[0,1]\n\t"
      "ds_read_b128 v[68:71], %[ra] offset:0\n\t"
      "v_pk_mul_f16 v78, v73, v73 op_sel_hi:[0,1]\n\t"
      "ds_read_b128 v[88:91], %[ra] offset:64\n\t"
      "v_pk_mul_f16 v79, v80, v73 op_sel_hi:[0,1]\n\t"
      "v_pk_fma_f16 v72, v73, v75, v72 op_sel:[1,1,0] op_sel_hi:[1,0,1] neg_lo:[0,1,0]\n\t"
      "ds_read_b128 v[92:95], %[ra] offset:128\n\t"
      "v_pk_fma_f16 v76, v73, v74, v76 op_sel:[1,1,0] op_sel_hi:[1,0,1] neg_lo:[0,1,0]\n\t"
      "v_pk_fma_f16 v81, v73, v77, v81 op_sel:[1,1,0] op_sel_hi:[1,0,1] neg_lo:[0,1,0]\n\t"
      "ds_read_b128 v[96:99], %[ra] offset:192\n\t"
      "v_pk_fma_f16 v78, v73, v73, v78 op_sel:[1,1,0] op_sel_hi:[1,0,1] neg_lo:[0,1,0]\n\t"
      "v_pk_fma_f16 v79, v80, v73, v79 op_sel:[1,1,0] op_sel_hi:[1,0,1] neg_lo:[0,1,0]\n\t"
      "ds_write_addtid_b32 v73 offset:0\n\t"
      "v_pk_mul_f16 v73, v80, v72 op_sel_hi:[0,1]\n\t"
      "ds_write_addtid_b32 v79 offset:288\n\t"
      "v_pk_mul_f16 v77, v80, v76 op_sel_hi:[0,1]\n\t"
      "v_pk_mul_f16 v79, v80, v81 op_sel_hi:[0,1]\n\t"
      "ds_write_addtid_b32 v72 offset:576\n\t"
      "v_pk_fma_f16 v73, v80, v72, v73 op_sel:[1,1,0] op_sel_hi:[1,0,1] neg_lo:[0,1,0]\n\t"
      "v_pk_fma_f16 v77, v80, v76, v77 op_sel:[1,1,0] op_sel_hi:[1,0,1] neg_lo:[0,1,0]\n\t"
      "ds_write_addtid_b32 v73 offset:864\n\t"
      "v_pk_fma_f16 v79, v80, v81, v79 op_sel:[1,1,0] op_sel_hi:[1,0,1] neg_lo:[0,1,0]\n\t"
      "ds_write_addtid_b32 v76 offset:1152\n\t"
      "ds_write_addtid_b32 v77 offset:1440\n\t"
      "ds_write_addtid_b32 v81 offset:1728\n\t"
      "ds_write_addtid_b32 v79 offset:2016\n\t"
      "ds_write_addtid_b32 v78 offset:2304\n\t"
      "ds_read_b128 v[72:75], %[ra] offset:0\n\t"
      "ds_read_b128 v[76:79], %[ra] offset:64\n\t"
      "ds_read_b128 v[80:83], %[ra] offset:128\n\t"
      "ds_read_b128 v[114:117], %[ra] offset:192\n\t"
      "s_waitcnt lgkmcnt(14)\n\t"
      "v_mfma_f32_16x16x32_f16 v[118:121], v[68:71], v[100:103], 0\n\t"
      "v_mfma_f32_16x16x32_f16 v[118:121], v[88:91], v[104:107], v[118:121]\n\t"
      "v_mfma_f32_16x16x32_f16 v[118:121], v[92:95], v[84:87], v[118:121]\n\t"
      "s_waitcnt lgkmcnt(13)\n\t"
      "v_mfma_f32_16x16x32_f16 v[118:121], v[96:99], v[110:113], v[118:121]\n\t"
      "s_waitcnt lgkmcnt(3)\n\t"
      "v_mfma_f32_16x16x32_f16 v[68:71], v[72:75], v[100:103], 0\n\t"
      "s_waitcnt lgkmcnt(2)\n\t"
      "v_mfma_f32_16x16x32_f16 v[68:71], v[76:79], v[104:107], v[68:71]\n\t"
      "s_waitcnt lgkmcnt(1)\n\t"
      "v_mfma_f32_16x16x32_f16 v[68:71], v[80:83], v[84:87], v[68:71]\n\t"
      "s_waitcnt lgkmcnt(0)\n\t"
      "v_mfma_f32_16x16x32_f16 v[68:71], v[114:117], v[110:113], v[68:71]\n\t"
      "v_mul_f32 v118, %[s0], v118\n\t"
      "v_mul_f32 v119, %[s0], v119\n\t"
      "v_mul_f32 v120, %[s0], v120\n\t"
      "v_mul_f32 v121, %[s0], v121\n\t"
      "ds_write_b128 %[pa], v[118:121] offset:4864\n\t"
      "s_nop 2\n\t"
      "v_mul_f32 v68, %[s0], v68\n\t"
      "v_mul_f32 v69, %[s0], v69\n\t"
      "v_mul_f32 v70, %[s0], v70\n\t"
      "v_mul_f32 v71, %[s0], v71\n\t"
      "ds_write_b128 %[pb], v[68:71] offset:0\n\t"
      "s_branch 79f\n\t"
      "70:\n\t"
      "s_mov_b32 %[s2], 0\n\t"
      "71:\n\t"
      "s_mov_b32 %[s3], 0\n\t"
      "v_mov_b32 v113, %[wa]\n\t"
      "72:\n\t"
      "s_lshl_b32 %[s0], %[s3], 1\n\t"
      "s_add_u32 %[s0], %[s0], 256\n\t"
      "s_and_b32 %[s1], %[s3], 3\n\t"
      "s_lshl_b32 %[s1], %[s1], 2\n\t"
      "s_and_b32 %[s4], %[s3], 4\n\t"
      "s_lshl_b32 %[s4], %[s4], 6\n\t"
      "s_add_u32 %[s1], %[s1], %[s4]\n\t"
      "s_add_u32 %[s1], %[s1], 512\n\t"
      "s_cmp_lt_u32 %[s3], 8\n\t"
      "s_cselect_b32 %[s0], %[s0], %[s1]\n\t"
      "s_and_b32 %[s1], %[s3], 1\n\t"
      "s_lshl_b32 %[s1], %[s1], 3\n\t"
      "s_lshr_b32 %[s4], %[s3], 1\n\t"
      "s_lshl_b32 %[s4], %[s4], 8\n\t"
      "s_add_u32 %[s1], %[s1], %[s4]\n\t"
      "s_add_u32 %[s1], %[s1], 1024\n\t"
      "s_cmp_lt_u32 %[s3], 8\n\t"
      "s_cselect_b32 %[s1], %[s1], 2048\n\t"
      "s_cmp_eq_u32 %[s2], 3\n\t"
      "s_cselect_b32 %[s0], %[s1], %[s0]\n\t"
      "s_cmp_eq_u32 %[s2], 1\n\t"
      "s_cselect_b32 %[s0], %[s3], %[s0]\n\t"
      "s_lshl_b32 %[s1], %[s3], 4\n\t"
      "s_cmp_eq_u32 %[s2], 0\n\t"
      "s_cselect_b32 %[s0], %[s1], %[s0]\n\t"
      "v_cvt_f32_u32 v104, %[s0]\n\t"
      "v_mul_f32 v105, %[a2], v104\n\t"
      "v_mul_f32 v106, %[b2], v104\n\t"
      "v_exp_f32 v105, v105\n\t"
      "v_fract_f32 v106, v106\n\t"
      "v_cos_f32 v107, v106\n\t"
      "v_sin_f32 v108, v106\n\t"
      "s_nop 0\n\t"
      "v_mul_f32 v107, v105, v107\n\t"
      "v_mul_f32 v108, v105, v108\n\t"
      "s_cbranch_scc0 73f\n\t"
      "v_mul_f32 v109, %[cy], v108\n\t"
      "v_mul_f32 v110, %[cy], v107\n\t"
      "v_fma_f32 v107, %[cx], v107, -v109\n\t"
      "v_fma_f32 v108, -%[cx], v108, -v110\n\t"
      "73:\n\t"
      "v_cvt_pk_bf16_f32 v107, v107, v108\n\t"
      "ds_write_b32 v113, v107\n\t"
      "v_add_u32 v113, 288, v113\n\t"
      "s_add_u32 %[s3], %[s3], 1\n\t"
      "s_cmp_lt_u32 %[s3], 16\n\t"
      "s_cbranch_scc1 72b\n\t"
      "s_cmp_eq_u32 %[s2], 0\n\t"
      "s_cbranch_scc0 74f\n\t"
      "ds_read_b128 v[68:71], %[ra] offset:0\n\t"
      "ds_read_b128 v[72:75], %[ra] offset:64\n\t"
      "ds_read_b128 v[76:79], %[ra] offset:128\n\t"
      "ds_read_b128 v[80:83], %[ra] offset:192\n\t"
      "s_branch 75f\n\t"
      "74:\n\t"
      "ds_read_b128 v[84:87], %[ra] offset:0\n\t"
      "ds_read_b128 v[88:91], %[ra] offset:64\n\t"
      "ds_read_b128 v[92:95], %[ra] offset:128\n\t"
      "ds_read_b128 v[96:99], %[ra] offset:192\n\t"
      "s_cmp_eq_u32 %[s2], 1\n\t"
      "s_cselect_b32 %[s0], 0, 4864\n\t"
      "v_add_u32 v112, %[s0], %[pa]\n\t"
      "s_cmp_eq_u32 %[s2], 3\n\t"
      "s_cbranch_scc0 76f\n\t"
      "v_mov_b32 v112, %[pb]\n\t"
      "76:\n\t"
      "s_waitcnt lgkmcnt(0)\n\t"
      "v_mfma_f32_16x16x32_bf16 v[100:103], v[84:87], v[68:71], 0\n\t"
      "v_mfma_f32_16x16x32_bf16 v[100:103], v[88:91], v[72:75], v[100:103]\n\t"
      "v_mfma_f32_16x16x32_bf16 v[100:103], v[92:95], v[76:79], v[100:103]\n\t"
      "v_mfma_f32_16x16x32_bf16 v[100:103], v[96:99], v[80:83], v[100:103]\n\t"
      "s_nop 7\n\t"
      "ds_write_b128 v112, v[100:103]\n\t"
      "s_cmp_eq_u32 %[s2], 1\n\t"
      "s_cbranch_scc0 75f\n\t"
      "ds_add_u32 %[fc], %[one] offset:96\n\t"
      "75:\n\t"
      "s_add_u32 %[s2], %[s2], 1\n\t"
      "s_cmp_lt_u32 %[s2], 4\n\t"
      "s_cbranch_scc1 71b\n\t"
      "79:\n\t"
      : [s0] "=&s"(s0), [s1] "=&s"(s1), [s2] "=&s"(s2), [s3] "=&s"(s3), [s4] "=&s"(s4)
      : [a2] "v"(a2), [b2] "v"(b2), [cx] "v"(cx), [cy] "v"(cy), [ra] "v"(ra), [pa] "v"(pa), [pb] "v"(pb), [wa] "v"(wa), [fc] "v"(fc), [one] "v"(1), [w] "s"(w)
      : "memory", "scc", "vcc", "v68", "v69", "v70", "v71", "v72", "v73", "v74", "v75", "v76", "v77", "v78", "v79", "v80", "v81", "v82", "v83", "v84", "v85", "v86", "v87", "v88", "v89", "v90", "v91", "v92", "v93", "v94", "v95", "v96", "v97", "v98", "v99", "v100", "v101", "v102", "v103", "v104", "v105", "v106", "v107", "v108", "v109", "v110", "v111", "v112", "v113", "v114", "v115", "v116", "v117", "v118", "v119", "v120", "v121");
}

DI void kgen_task(const Raw& r, float dl, float dp, float dc, int br, int wa, int ra, int pa, int pb, int fc, int w) {
  float a2, cx, cy;
  if (br == 0) {
    a2 = -rbf(__builtin_amdgcn_exp2f(r.ar * 1.44269504088896341f)) * dl;
    cx = r.cr * dc; cy = r.ci * dc;
  } else {
    a2 = r.ar * dl;
    const float bx = r.br * dc, by = r.bi * dc;
    cx = r.cr * bx - r.ci * by; cy = r.cr * by + r.ci * bx;
  }
  kgen_tables(a2, r.ai * dp, cx, cy, wa, ra, pa, pb, fc, w);
}

template <int OFF> DI void wr64(int a, unsigned x, unsigned y) { asm volatile("ds_write_b64 %0, %1 offset:%2" :: "v"(a), "v"(u2{x, y}), "i"(OFF) : "memory"); }
DI u4 band(int lane, int lo, int n) {
  unsigned m = (((1u << n) - 1u) << lo << (lane & 15)) >> (8 * (lane >> 4));
  u4 r;
#pragma unroll
  for (int j = 0; j < 4; j++)
    r[j] = ((unsigned)__builtin_amdgcn_sbfe(m, 2 * j, 1) & 0xffffu) | ((unsigned)__builtin_amdgcn_sbfe(m, 2 * j + 1, 1) << 16);
  return r;
}

#define CODE_LINES 243
#define GAS __attribute__((address_space(1)))
#define ENDPGM do { __builtin_amdgcn_endpgm(); __builtin_unreachable(); } while (0)
DI int lane_id() { int l; asm volatile("v_mbcnt_lo_u32_b32 %0, -1, 0\n\tv_mbcnt_hi_u32_b32 %0, -1, %0" : "=v"(l)); return l; }
#define LD6(r, o) asm volatile("global_load_short_d16_hi %0, %6, %7\n\tglobal_load_short_d16_hi %1, %6, %8\n\tglobal_load_short_d16_hi %2, %6, %9\n\t" \
    "global_load_short_d16_hi %3, %6, %10\n\tglobal_load_short_d16_hi %4, %6, %11\n\tglobal_load_short_d16_hi %5, %6, %12" \
    : "+v"(r.ar), "+v"(r.ai), "+v"(r.cr), "+v"(r.ci), "+v"(r.br), "+v"(r.bi) \
    : "v"(o), "s"(B.are), "s"(B.aim), "s"(B.cre), "s"(B.cim), "s"(B.bre), "s"(B.bim) : "memory")
DI void wait_flag(int a, unsigned n) {
  unsigned t, cnt = 1u << 20, v;
  asm volatile("1:\n\tds_read_b32 %2, %3\n\ts_waitcnt lgkmcnt(0)\n\tv_readfirstlane_b32 %0, %2\n\ts_sub_u32 %1, %1, 1\n\ts_cbranch_scc1 2f\n\t"
               "s_and_b32 %0, %0, 63\n\ts_cmp_lt_u32 %0, %4\n\ts_cbranch_scc0 2f\n\ts_sleep 1\n\ts_branch 1b\n\t2:"
               : "=&s"(t), "+s"(cnt), "=&v"(v) : "v"(a), "s"(n) : "memory", "scc");
}

__global__ __launch_bounds__(1024, 4) void ssm_fused(Params p) {
  __shared__ __attribute__((aligned(16))) char smem[LDS_BYTES];
  asm volatile("" :: "v"((__attribute__((address_space(3))) char*)smem));
  const int w = __builtin_amdgcn_readfirstlane(threadIdx.x >> 6);
  __builtin_amdgcn_s_setprio(3);
  unsigned cpf = 0;
  if (threadIdx.x < CODE_LINES) {
    unsigned long long pc;
    asm volatile("s_getpc_b64 %0" : "=s"(pc));
    asm volatile("global_load_dword %0, %1, %2" : "=v"(cpf) : "v"(threadIdx.x * 64u), "s"(pc & ~63ull) : "memory");
  }
  typedef unsigned long long q8 __attribute__((ext_vector_type(8)));
  q8 ka0; unsigned long long ka1, ka2;
  if (threadIdx.x < 32) *LDSP(unsigned, OFF_FLAG + threadIdx.x * 4) = 0u;
  asm volatile("s_waitcnt lgkmcnt(0)\n\ts_load_dwordx16 %0, %3, %4\n\ts_load_dwordx2 %1, %3, %5\n\ts_load_dwordx2 %2, %3, 0x90\n\ts_barrier"
               : "=&s"(ka0), "=&s"(ka1), "=&s"(ka2)
               : "s"(__builtin_amdgcn_kernarg_segment_ptr()), "s"((blockIdx.x & 4) * 18), "s"((blockIdx.x & 4) * 18 + 64) : "memory");
  const int bx = blockIdx.x;
  const int by = blockIdx.y, sw = (bx & 1) ? 0x190c1306 : 0x1306190c;
  const int bj = by < 4 ? (sw >> (8 * by)) & 31 : by == (sw & 31) ? 0 : by == ((sw >> 8) & 31) ? 1 : by == ((sw >> 16) & 31) ? 2 : by == (sw >> 24) ? 3 : by;
  constexpr int nch = 5;
  const int br = bx >> 2;
  const int e0 = (bx & 3) * 160 + 5 * bj;
  const int tk0 = w - (w >> 2 == 3);
  const int c0 = (tk0 * 11) >> 5;
  unsigned q[4];
  Raw r0 = {0.f, 0.f, 0.f, 0.f, 0.f, 0.f};
  {
    const unsigned lane = lane_id();
    if (w == 12) {
      const unsigned base = (bj + 32u * (lane >> 2)) * 1280u + (((bx & 3) * 320u) & ~127u) + min(lane & 3u, 2u) * 128u;
#pragma unroll
      for (int i = 0; i < 4; i++) q[i] = base + i * 655360u;
    } else {
      q[0] = (e0 * NMODE + tk0 * 64 + lane) * 2u;
    }
  }
  const unsigned lo0 = (e0 + c0) & ~1u;
  asm volatile("s_waitcnt lgkmcnt(0)" : "+s"(ka0), "+s"(ka1), "+s"(ka2), "+v"(q[0]), "+v"(q[1]), "+v"(q[2]), "+v"(q[3]),
               "+v"(r0.ar), "+v"(r0.ai), "+v"(r0.cr), "+v"(r0.ci), "+v"(r0.br), "+v"(r0.bi),
               "+v"(cpf) : "s"(lo0 * 2) : "memory");
  const Br B = {(const char*)ka0[0], (const char*)ka0[1], (const char*)ka0[2], (const char*)ka0[3], (const char*)ka0[4],
                (const char*)ka0[5], (const char*)ka0[6], (const char*)ka0[7], (const char*)ka1};
  char* const outp = (char*)ka2;

  const int c = 0x3210440032143210ull >> (4 * w) & 15;
  const bool far = (0x6e0 >> w) & 1;
  const int kn = OFF_PK + c * PKS;
  unsigned R[28];
  if (w == 12) {
    unsigned pf[4];
#pragma unroll
    for (int i = 0; i < 4; i++) asm volatile("global_load_dword %0, %1, %2" : "=v"(pf[i]) : "v"(q[i]), "s"(B.u) : "memory");
    SB();
    const int lane = lane_id();
    *LDSP(u4, OFF_BAND + lane * 16) = band(lane, 5, 7) & 0x3f803f80u;
    *LDSP(u4, OFF_BAND + 1024 + lane * 16) = band(lane, 4, 9) & 0x3f803f80u;
    const int fa = OFF_FLAG + (lane << 24) + 4 * lane;
    for (int i = lane; i < 5 * 36; i += 64) *LDSP(u4, OFF_UT + (i / 36) * UTS + (i % 36) * 16) = u4{0, 0, 0, 0};
    const bool odd = bj & 1;
    const unsigned so = lane * 5120u + ((e0 * 2u) & ~3u);
    const int ua = OFF_UT + (UT_PAD + 4 * lane) * 2 - (odd ? UTS : 0);
    u3 ur[8][4];
#pragma unroll
    for (int t = 0; t < 10; t++) {
      if (t < 8) {
        if (t == 0) asm volatile("s_waitcnt vmcnt(3)" : "+v"(pf[0]) :: "memory");
        if (t == 1) asm volatile("s_waitcnt vmcnt(6)" : "+v"(pf[1]) :: "memory");
        if (t == 2) asm volatile("s_waitcnt vmcnt(8)" : "+v"(pf[2]), "+v"(pf[3]) :: "memory");
#pragma unroll
        for (int r = 0; r < 4; r++) ur[t][r] = *(const GAS u3*)((const GAS char*)B.u + (so + (t * 256 + r) * 1280u));
      }
      SB();
      if (t >= 2) {
        const int h = t - 2;
#pragma unroll
        for (int s = 0; s < 6; s++) {
          const unsigned sel = (s & 1) ? 0x07060302u : 0x05040100u;
          if ((s != 0 || !odd) && (s != 5 || odd))
            wr64<0>(ua + h * 512 + s * UTS, __builtin_amdgcn_perm(ur[h][1][s >> 1], ur[h][0][s >> 1], sel), __builtin_amdgcn_perm(ur[h][3][s >> 1], ur[h][2][s >> 1], sel));
        }
        asm volatile("ds_add_u32 %0, %1 offset:%2" :: "v"(fa), "v"(h < 4 ? 1 : 0x10000), "i"(h < 4 ? 60 : 4 * (h - 4)) : "memory");
        SB();
      }
    }
  } else {
    const int lane = lane_id(), la = lane & 15, lg = lane >> 4;
    LD6(r0, q[0]);
    const unsigned l0 = *CSTP(unsigned, B.ldt + lo0 * 2);
    __builtin_amdgcn_s_setprio(1);
    if (w < 5) {
      R[0] = OFF_UT + c * UTS + UT_PAD * 2 + la * 32 + lg * 16 - 32 * 17;
      R[1] = w * (SCR + 64) + la * 32 + lg * 8;
      R[2] = (0xa7659 >> (4 * c) & 15) * SCR + lane * 16;
      R[3] = kn + ((la + 1) & 3) * (KNC + 2) + (543 - 16 - 512 - la + 8 * lg) * 2;
      R[8] = kn + pslot(lane & 3, lane >> 2);
      const unsigned dk = CSTP(unsigned, B.dsk)[(e0 + c) >> 1];
      R[9] = lane == 0 ? (((e0 + c) & 1) ? dk & 0xffff0000u : dk << 16) : 0u;
      R[10] = kn + (71 - lane) * 8;
      R[11] = kn + lg * KNC + (la < 7 ? la : (la < 12 ? 129 + la : 140)) * 8;
      R[12] = kn + 136 * 8 + (lane << 24) + 4 * lane;
      R[20] = kn + lg * KNC + (64 + (la & 7)) * 8;
      R[21] = lane == 63 ? OFF_G63 + 16 * c : (1 << 30) + 16 * lane;
    } else if (far) {
      const int kf = kn + 7936;
      R[0] = OFF_UT + c * UTS + UT_PAD * 2 + la * 32 + lg * 16 - 528;
      R[1] = OFF_BAND + lane * 16;
      R[2] = w * SCR + lane * 16;
      R[3] = kf + (15 - la + 4 * lg) * 4;
      const u4 b0 = band(lane, 11, 3) & 0x3f803f80u, b1 = band(lane, 6, 5) & 0x3f803f80u, b2 = band(lane, 3, 3) & 0x3f803f80u,
               b3 = band(lane, 4, 7);
#pragma unroll
      for (int i = 0; i < 4; i++) { R[4 + i] = b0[i]; R[8 + i] = b1[i]; R[12 + i] = b2[i]; R[16 + i] = b3[i]; }
      R[20] = kf + pslot(lane >> 5, la) + (lg & 1) * 8;
      R[21] = kn + 4864 + pslot(2 + (lg & 1), la);
      R[22] = kf + 512 + (63 - lane) * 4;
      R[23] = kf + 512 + (lane == 0 ? 80 : 63 + la + (la == 0)) * 4;
      R[24] = kf + (lane < 32 ? 47 - lane : (lane < 48 ? lane - 32 : lane)) * 4;
      R[25] = lane == 0 ? ~0u : 0u;
      R[26] = lane < 32 ? ~0u : 0u;
      R[27] = lane < 31 ? ~0u : (lane == 31 ? 0xffffu : 0u);
    } else {
      R[0] = kn + 4864 + pslot((lane >> 1) & 1, lane >> 2) + (lane & 1) * 8;
      R[1] = kn + (71 - lane) * 8;
      R[2] = lane < 4 ? kn + lane * KNC + 7 * 8 : (lane << 24) + 8 * lane;
      R[3] = lane == 0 ? 0xffff0000u : 0u;
      R[4] = lane < 2 ? ~0u : (lane == 2 ? 0xffff0000u : 0u);
      R[5] = kn + 72 * 8 + (lane << 24) + 8 * lane;
      R[6] = OFF_G63 + 16 * c;
    }
    asm volatile("" : "+v"(R[0]), "+v"(R[1]), "+v"(R[2]), "+v"(R[3]), "+v"(R[4]), "+v"(R[5]), "+v"(R[6]), "+v"(R[7]), "+v"(R[8]), "+v"(R[9]), "+v"(R[10]), "+v"(R[11]), "+v"(R[12]), "+v"(R[13]), "+v"(R[14]), "+v"(R[15]), "+v"(R[16]), "+v"(R[17]), "+v"(R[18]), "+v"(R[19]), "+v"(R[20]), "+v"(R[21]), "+v"(R[22]), "+v"(R[23]), "+v"(R[24]), "+v"(R[25]), "+v"(R[26]), "+v"(R[27]));
    __builtin_amdgcn_s_setprio(3);
    const int scr = __builtin_amdgcn_readfirstlane(tk0 * SCR);
    set_m0(scr);
    int ra = scr + la * ROWB + lg * 16, wa = scr + lane * 4;
    int pa = OFF_PK + c0 * PKS + (tk0 - 3 * c0) * 1024 + pslot(lg, la);
    int pb = OFF_PK + c0 * PKS + 7936 + (tk0 - 3 * c0) * 544 + (lane <= 32 ? (lane < 32 ? pslot(lg, la) : 512) : (1 << 30) + 16 * lane);
    int fc = OFF_FLAG + (lane << 24) + 4 * lane + 4 * c0;
    const float dt = rbf(exp_acc(((e0 + c0) & 1) ? bfhi(l0) : bflo(l0)));
    float dl = dt * 1.44269504088896341f, dp = dt * 0.15915494309189535f, dc = br ? dt : dt + dt;
    asm volatile("s_waitcnt vmcnt(0)" : "+v"(cpf), "+v"(r0.ar), "+v"(r0.ai), "+v"(r0.cr), "+v"(r0.ci), "+v"(r0.br), "+v"(r0.bi),
                 "+v"(dl), "+v"(dp), "+v"(dc), "+v"(ra), "+v"(pa), "+v"(wa), "+v"(fc), "+v"(pb) :: "memory");
    kgen_task(r0, dl, dp, dc, br, wa, ra, pa, pb, fc, w);
    asm volatile("ds_add_u32 %0, %1 offset:64" :: "v"(fc), "v"(1) : "memory");
  }
  __builtin_amdgcn_s_setprio(0);

  unsigned cnt = 1u << 20, t0, t1;
  if (w >= 5 && !far) {
  if (w != 12) {
    __builtin_amdgcn_s_setprio(3);
      asm volatile(
      "v_mov_b32 v64, %[fl]\n\t"
      "v_mov_b32 v65, %[kd]\n\t"
      "v_mov_b32 v66, %[pc]\n\t"
      "v_mov_b32 v67, %[bit]\n\t"
      "v_mbcnt_lo_u32_b32 v68, -1, 0\n\t"
      "v_mbcnt_hi_u32_b32 v68, -1, v68\n\t"
      "v_lshl_add_u32 v69, v68, 2, v64\n\t"
      "v_lshl_add_u32 v69, v68, 24, v69\n\t"
      "10:\n\t"
      "ds_read_b32 v70, v65\n\t"
      "ds_read_b32 v71, v64 offset:120\n\t"
      "s_waitcnt lgkmcnt(0)\n\t"
      "v_readfirstlane_b32 %[t0], v70\n\t"
      "v_readfirstlane_b32 %[t1], v71\n\t"
      "s_sub_u32 %[cnt], %[cnt], 1\n\t"
      "s_cbranch_scc1 11f\n\t"
      "s_cmp_lt_u32 %[t0], 3\n\t"
      "s_cselect_b32 %[t1], 0, %[t1]\n\t"
      "s_and_b32 %[t1], %[t1], %[bit]\n\t"
      "s_cbranch_scc1 11f\n\t"
      "s_sleep 1\n\t"
      "s_branch 10b\n\t"
      "11:\n\t"
      "ds_read_b64 v[72:73], v0 offset:0\n\t"
      "ds_read_b64 v[74:75], v0 offset:1024\n\t"
      "ds_read_b64 v[76:77], v0 offset:2048\n\t"
      "ds_read_b32 v78, v66 offset:640\n\t"
      "ds_read_b32 v79, v66 offset:1664\n\t"
      "ds_read_b32 v80, v66 offset:2688\n\t"
      "ds_read_b128 v[84:87], v6\n\t"
      "s_waitcnt lgkmcnt(4)\n\t"
      "v_add_f32 v72, v72, v74\n\t"
      "v_add_f32 v73, v73, v75\n\t"
      "v_add_f32 v72, v72, v76\n\t"
      "v_add_f32 v73, v73, v77\n\t"
      "s_waitcnt lgkmcnt(1)\n\t"
      "v_add_f32 v78, v78, v79\n\t"
      "v_add_f32 v78, v78, v80\n\t"
      "v_mov_b32 v79, v78\n\t"
      "v_cvt_pk_bf16_f32 v80, v78, v78\n\t"
      "v_mov_b32_dpp v78, v72 wave_shl:1 row_mask:0xf bank_mask:0xf\n\t"
      "v_mov_b32_dpp v79, v73 wave_shl:1 row_mask:0xf bank_mask:0xf\n\t"
      "v_cvt_pk_bf16_f32 v88, v78, v73\n\t"
      "v_cvt_pk_bf16_f32 v89, v72, v72\n\t"
      "ds_write_b64 v1, v[88:89] offset:0\n\t"
      "v_cvt_pk_bf16_f32 v90, v78, v78\n\t"
      "v_cvt_pk_bf16_f32 v91, v73, v72\n\t"
      "ds_write_b64 v1, v[90:91] offset:1216\n\t"
      "v_cvt_pk_bf16_f32 v92, v78, v78\n\t"
      "v_cvt_pk_bf16_f32 v93, v78, v73\n\t"
      "ds_write_b64 v1, v[92:93] offset:2432\n\t"
      "v_cvt_pk_bf16_f32 v94, v79, v78\n\t"
      "v_cvt_pk_bf16_f32 v95, v78, v78\n\t"
      "ds_write_b64 v1, v[94:95] offset:3648\n\t"
      "v_and_b32 v82, v3, v80\n\t"
      "v_and_b32 v83, v4, v80\n\t"
      "s_waitcnt lgkmcnt(4)\n\t"
      "ds_write_b64 v2, v[82:83]\n\t"
      "v_cvt_pk_bf16_f32 v98, v72, v87\n\t"
      "v_cvt_pk_bf16_f32 v99, v86, v85\n\t"
      "ds_write_b64 v5, v[98:99] offset:1216\n\t"
      "v_cvt_pk_bf16_f32 v100, v72, v72\n\t"
      "v_cvt_pk_bf16_f32 v101, v87, v86\n\t"
      "ds_write_b64 v5, v[100:101] offset:2432\n\t"
      "v_cvt_pk_bf16_f32 v102, v73, v72\n\t"
      "v_cvt_pk_bf16_f32 v103, v72, v87\n\t"
      "ds_write_b64 v5, v[102:103] offset:3648\n\t"
      "ds_add_u32 v69, v67 offset:124\n\t"
      : [cnt] "+s"(cnt), [t0] "=&s"(t0), [t1] "=&s"(t1)
      : [fl] "s"(OFF_FLAG), [kd] "s"(OFF_FLAG + 64 + 4 * c), [pc] "s"(kn + 4864), [bit] "s"(1 << c),
        "{v0}"(R[0]), "{v1}"(R[1]), "{v2}"(R[2]), "{v3}"(R[3]), "{v4}"(R[4]), "{v5}"(R[5]), "{v6}"(R[6])
      : "memory", "scc", "v64", "v65", "v66", "v67", "v68", "v69", "v70", "v71", "v72", "v73", "v74", "v75", "v76", "v77", "v78", "v79", "v80", "v81", "v82", "v83", "v84", "v85", "v86", "v87", "v88", "v89", "v90", "v91", "v92", "v93", "v94", "v95", "v96", "v97", "v98", "v99", "v100", "v101", "v102", "v103");
  }
  if (w == 8 || w > 12) {
    __builtin_amdgcn_s_setprio(3);
    const int lane = lane_id();
    const unsigned row = (w & 1) * 256 + (w & 2) * 64 + (lane >> 3) * 4, cc = lane & 7;
    int ya = cc * (SCR + 64) + row * 2;
    const unsigned oo = row * 2560u + (e0 + br * 640 + cc) * 2u;
    unsigned long long ob = (unsigned long long)outp;
    if (cc < 5) {
#pragma nounroll
      for (int t = w & 1; t < 8; t += 2) {
        wait_flag(OFF_FLAG + 4 * t, 31);
        u2 y[4];
#pragma unroll
        for (int i = 0; i < 4; i++) y[i] = *LDSP(u2, ya + 64 * i);
        ya += 1024;
        asm volatile("" : "+v"(ya));
#pragma unroll
        for (int i = 0; i < 16; i++) {
          const unsigned long long o = ob + ((i & 3) + (i >> 2) * 32) * 2560ull;
          if (i & 1) asm volatile("global_store_short_d16_hi %0, %1, %2" :: "v"(oo), "v"(y[i >> 2][(i >> 1) & 1]), "s"(o) : "memory");
          else asm volatile("global_store_short %0, %1, %2" :: "v"(oo), "v"(y[i >> 2][(i >> 1) & 1]), "s"(o) : "memory");
        }
        ob += 512 * 2560;
      }
    }
  }
  ENDPGM;
  }

  if (w < 5) {
    asm volatile(
      "s_setprio 1\n\t"
      "v_mov_b32 v122, v20\n\t"
      "v_mov_b32 v121, v10\n\t"
      "v_mov_b32 v4, %[ff]\n\t"
      "v_mov_b32 v13, %[fl]\n\t"
      "v_mbcnt_lo_u32_b32 v7, -1, 0\n\t"
      "v_mbcnt_hi_u32_b32 v7, -1, v7\n\t"
      "v_mov_b32 v6, %[bit]\n\t"
      "v_lshl_add_u32 v5, v7, 2, %[fl]\n\t"
      "v_lshl_add_u32 v5, v7, 24, v5\n\t"
      "v_mov_b32 v126, %[fl]\n\t"
      "v_mov_b32 v124, v21\n\t"
      "v_mov_b32 v14, 0\n\t"
      "v_mov_b32 v15, 0\n\t"
      "110:\n\t"
      "ds_read_b32 v7, v13 offset:60\n\t"
      "s_waitcnt lgkmcnt(0)\n\t"
      "s_sub_u32 %[cnt], %[cnt], 1\n\t"
      "s_cbranch_scc1 111f\n\t"
      "v_cmp_le_u32 vcc, 1, v7\n\t"
      "s_cbranch_vccnz 111f\n\t"
      "s_sleep 2\n\t"
      "s_branch 110b\n\t"
      "111:\n\t"
      "ds_read_b128 v[20:23], v0 offset:0\n\t"
      "ds_read_b128 v[24:27], v0 offset:64\n\t"
      "ds_read_b128 v[28:31], v0 offset:128\n\t"
      "ds_read_b128 v[32:35], v0 offset:192\n\t"
      "ds_read_b128 v[36:39], v0 offset:256\n\t"
      "ds_read_b128 v[40:43], v0 offset:320\n\t"
      "ds_read_b128 v[44:47], v0 offset:384\n\t"
      "ds_read_b128 v[48:51], v0 offset:448\n\t"
      "ds_read_b128 v[52:55], v0 offset:512\n\t"
      "100:\n\t"
      "ds_read_b32 v7, v4 offset:64\n\t"
      "s_waitcnt lgkmcnt(0)\n\t"
      "s_sub_u32 %[cnt], %[cnt], 1\n\t"
      "s_cbranch_scc1 101f\n\t"
      "v_cmp_le_u32 vcc, 3, v7\n\t"
      "s_cbranch_vccnz 101f\n\t"
      "s_sleep 1\n\t"
      "s_branch 100b\n\t"
      "101:\n\t"
      "s_cmp_lg_u32 %[bit], 16\n\t"
      "s_cbranch_scc1 102f\n\t"
      "s_setprio 3\n\t"
      "102:\n\t"
      "ds_read_b128 v[92:95], v8 offset:0\n\t"
      "ds_read_b128 v[96:99], v8 offset:1024\n\t"
      "ds_read_b128 v[100:103], v8 offset:2048\n\t"
      "ds_write_b64 v11, v[14:15]\n\t"
      "ds_write_b64 v122, v[14:15]\n\t"
      "s_waitcnt lgkmcnt(4)\n\t"
      "v_add_f32 v92, v92, v9\n\t"
      "s_waitcnt lgkmcnt(3)\n\t"
      "v_add_f32 v92, v92, v96\n\t"
      "v_add_f32 v93, v93, v97\n\t"
      "v_add_f32 v94, v94, v98\n\t"
      "v_add_f32 v95, v95, v99\n\t"
      "s_waitcnt lgkmcnt(2)\n\t"
      "v_add_f32 v92, v92, v100\n\t"
      "v_add_f32 v93, v93, v101\n\t"
      "v_add_f32 v94, v94, v102\n\t"
      "v_add_f32 v95, v95, v103\n\t"
      "v_cvt_pk_bf16_f32 v110, v95, v94\n\t"
      "v_cvt_pk_bf16_f32 v111, v93, v92\n\t"
      "v_mov_b32_dpp v103, v92 wave_shl:1 row_mask:0xf bank_mask:0xf bound_ctrl:1\n\t"
      "ds_write_b64 v10, v[110:111] offset:512\n\t"
      "v_mov_b32_dpp v104, v93 wave_shl:1 row_mask:0xf bank_mask:0xf bound_ctrl:1\n\t"
      "v_mov_b32_dpp v105, v94 wave_shl:1 row_mask:0xf bank_mask:0xf bound_ctrl:1\n\t"
      "v_cvt_pk_bf16_f32 v112, v103, v95\n\t"
      "v_cvt_pk_bf16_f32 v113, v94, v93\n\t"
      "ds_write_b64 v121, v[112:113] offset:1728\n\t"
      "v_cvt_pk_bf16_f32 v114, v104, v103\n\t"
      "v_cvt_pk_bf16_f32 v115, v95, v94\n\t"
      "ds_write_b64 v121, v[114:115] offset:2944\n\t"
      "v_cvt_pk_bf16_f32 v116, v105, v104\n\t"
      "v_cvt_pk_bf16_f32 v117, v103, v95\n\t"
      "ds_write_b64 v121, v[116:117] offset:4160\n\t"
      "v_cvt_pk_bf16_f32 v109, v92, 0\n\t"
      "ds_write_b128 v124, v[92:95]\n\t"
      "ds_add_u32 v5, v6 offset:120\n\t"
      "ds_write_b32 v12, v111 offset:2432\n\t"
      "ds_write_b32 v12, v113 offset:3648\n\t"
      "ds_write_b32 v12, v109 offset:1216\n\t"
      "ds_write_b32 v12, v109 offset:3652\n\t"
      "ds_read_b64 v[88:89], v3 offset:512\n\t"
      "ds_read_b64 v[90:91], v3 offset:520\n\t"
      "ds_read_b64 v[84:85], v3 offset:576\n\t"
      "ds_read_b64 v[86:87], v3 offset:584\n\t"
      "ds_read_b64 v[80:81], v3 offset:640\n\t"
      "ds_read_b64 v[82:83], v3 offset:648\n\t"
      "ds_read_b64 v[76:77], v3 offset:704\n\t"
      "ds_read_b64 v[78:79], v3 offset:712\n\t"
      "ds_read_b64 v[72:73], v3 offset:768\n\t"
      "ds_read_b64 v[74:75], v3 offset:776\n\t"
      "ds_read_b64 v[68:69], v3 offset:832\n\t"
      "ds_read_b64 v[70:71], v3 offset:840\n\t"
      "s_waitcnt lgkmcnt(10)\n\t"
      "v_mfma_f32_16x16x32_bf16 v[8:11], v[88:91], v[20:23], 0\n\t"
      "ds_read_b64 v[64:65], v3 offset:896\n\t"
      "ds_read_b64 v[66:67], v3 offset:904\n\t"
      "s_waitcnt lgkmcnt(10)\n\t"
      "v_mfma_f32_16x16x32_bf16 v[8:11], v[84:87], v[24:27], v[8:11]\n\t"
      "ds_read_b64 v[60:61], v3 offset:960\n\t"
      "ds_read_b64 v[62:63], v3 offset:968\n\t"
      "s_waitcnt lgkmcnt(10)\n\t"
      "v_mfma_f32_16x16x32_bf16 v[8:11], v[80:83], v[28:31], v[8:11]\n\t"
      "ds_read_b64 v[56:57], v3 offset:1024\n\t"
      "ds_read_b64 v[58:59], v3 offset:1032\n\t"
      "s_waitcnt lgkmcnt(10)\n\t"
      "v_mfma_f32_16x16x32_bf16 v[8:11], v[76:79], v[32:35], v[8:11]\n\t"
      "s_waitcnt lgkmcnt(8)\n\t"
      "v_mfma_f32_16x16x32_bf16 v[8:11], v[72:75], v[36:39], v[8:11]\n\t"
      "s_waitcnt lgkmcnt(6)\n\t"
      "v_mfma_f32_16x16x32_bf16 v[8:11], v[68:71], v[40:43], v[8:11]\n\t"
      "s_waitcnt lgkmcnt(4)\n\t"
      "v_mfma_f32_16x16x32_bf16 v[8:11], v[64:67], v[44:47], v[8:11]\n\t"
      "s_waitcnt lgkmcnt(2)\n\t"
      "v_mfma_f32_16x16x32_bf16 v[8:11], v[60:63], v[48:51], v[8:11]\n\t"
      "s_waitcnt lgkmcnt(0)\n\t"
      "v_mfma_f32_16x16x32_bf16 v[8:11], v[56:59], v[52:55], v[8:11]\n\t"
      "s_nop 7\n\t"
      "v_cvt_pk_bf16_f32 v8, v8, v9\n\t"
      "v_cvt_pk_bf16_f32 v9, v10, v11\n\t"
      "ds_write_b64 v1, v[8:9] offset:0\n\t"
      "ds_add_u32 v5, v6\n\t"
      "s_setprio 0\n\t"
      "130:\n\t"
      "ds_read_b32 v7, v13 offset:124\n\t"
      "ds_read_b32 v125, v13 offset:60\n\t"
      "s_waitcnt lgkmcnt(0)\n\t"
      "v_readfirstlane_b32 %[t0], v7\n\t"
      "s_sub_u32 %[cnt], %[cnt], 1\n\t"
      "s_cbranch_scc1 131f\n\t"
      "s_and_b32 %[t0], %[t0], %[bit]\n\t"
      "s_cbranch_scc1 131f\n\t"
      "s_sleep 1\n\t"
      "s_branch 130b\n\t"
      "131:\n\t"
      "s_setprio 2\n\t"
      "s_cmp_lg_u32 %[bit], 16\n\t"
      "s_cbranch_scc1 132f\n\t"
      "s_setprio 3\n\t"
      "132:\n\t"
      "ds_read_b64 v[120:121], v3 offset:0\n\t"
      "ds_read_b64 v[122:123], v3 offset:8\n\t"
      "ds_read_b64 v[116:117], v3 offset:64\n\t"
      "ds_read_b64 v[118:119], v3 offset:72\n\t"
      "ds_read_b64 v[112:113], v3 offset:128\n\t"
      "ds_read_b64 v[114:115], v3 offset:136\n\t"
      "ds_read_b64 v[108:109], v3 offset:192\n\t"
      "ds_read_b64 v[110:111], v3 offset:200\n\t"
      "ds_read_b64 v[104:105], v3 offset:256\n\t"
      "ds_read_b64 v[106:107], v3 offset:264\n\t"
      "ds_read_b64 v[100:101], v3 offset:320\n\t"
      "ds_read_b64 v[102:103], v3 offset:328\n\t"
      "ds_read_b64 v[96:97], v3 offset:384\n\t"
      "ds_read_b64 v[98:99], v3 offset:392\n\t"
      "ds_read_b64 v[92:93], v3 offset:448\n\t"
      "ds_read_b64 v[94:95], v3 offset:456\n\t"
      "ds_read_b64 v[88:89], v3 offset:512\n\t"
      "ds_read_b64 v[90:91], v3 offset:520\n\t"
      "v_cmp_gt_u32 vcc, 4, v125\n\t"
      "s_cbranch_vccnz 140f\n\t"
      "141:\n\t"
      "s_waitcnt lgkmcnt(14)\n\t"
      "v_mfma_f32_16x16x32_bf16 v[12:15], v[120:123], v[20:23], 0\n\t"
      "v_mfma_f32_16x16x32_bf16 v[12:15], v[116:119], v[24:27], v[12:15]\n\t"
      "ds_read_b128 v[20:23], v0 offset:576\n\t"
      "s_waitcnt lgkmcnt(13)\n\t"
      "v_mfma_f32_16x16x32_bf16 v[12:15], v[112:115], v[28:31], v[12:15]\n\t"
      "ds_read_b128 v[24:27], v0 offset:640\n\t"
      "s_waitcnt lgkmcnt(12)\n\t"
      "v_mfma_f32_16x16x32_bf16 v[12:15], v[108:111], v[32:35], v[12:15]\n\t"
      "ds_read_b128 v[28:31], v0 offset:704\n\t"
      "s_waitcnt lgkmcnt(11)\n\t"
      "v_mfma_f32_16x16x32_bf16 v[12:15], v[104:107], v[36:39], v[12:15]\n\t"
      "ds_read_b128 v[32:35], v0 offset:768\n\t"
      "s_waitcnt lgkmcnt(10)\n\t"
      "v_mfma_f32_16x16x32_bf16 v[12:15], v[100:103], v[40:43], v[12:15]\n\t"
      "ds_read_b128 v[36:39], v0 offset:832\n\t"
      "s_waitcnt lgkmcnt(9)\n\t"
      "v_mfma_f32_16x16x32_bf16 v[12:15], v[96:99], v[44:47], v[12:15]\n\t"
      "ds_read_b128 v[40:43], v0 offset:896\n\t"
      "s_waitcnt lgkmcnt(8)\n\t"
      "v_mfma_f32_16x16x32_bf16 v[12:15], v[92:95], v[48:51], v[12:15]\n\t"
      "ds_read_b128 v[44:47], v0 offset:960\n\t"
      "s_waitcnt lgkmcnt(7)\n\t"
      "v_mfma_f32_16x16x32_bf16 v[12:15], v[88:91], v[52:55], v[12:15]\n\t"
      "ds_read_b128 v[48:51], v0 offset:1024\n\t"
      "v_mfma_f32_16x16x32_bf16 v[16:19], v[120:123], v[52:55], 0\n\t"
      "s_waitcnt lgkmcnt(6)\n\t"
      "v_mfma_f32_16x16x32_bf16 v[12:15], v[84:87], v[20:23], v[12:15]\n\t"
      "ds_read_b128 v[52:55], v0 offset:1088\n\t"
      "v_mfma_f32_16x16x32_bf16 v[16:19], v[116:119], v[20:23], v[16:19]\n\t"
      "v_mfma_f32_16x16x32_bf16 v[12:15], v[80:83], v[24:27], v[12:15]\n\t"
      "ds_read_b128 v[20:23], v0 offset:1152\n\t"
      "v_mfma_f32_16x16x32_bf16 v[16:19], v[112:115], v[24:27], v[16:19]\n\t"
      "s_waitcnt lgkmcnt(6)\n\t"
      "v_mfma_f32_16x16x32_bf16 v[12:15], v[76:79], v[28:31], v[12:15]\n\t"
      "ds_read_b128 v[24:27], v0 offset:1216\n\t"
      "v_mfma_f32_16x16x32_bf16 v[16:19], v[108:111], v[28:31], v[16:19]\n\t"
      "v_mfma_f32_16x16x32_bf16 v[12:15], v[72:75], v[32:35], v[12:15]\n\t"
      "ds_read_b128 v[28:31], v0 offset:1280\n\t"
      "v_mfma_f32_16x16x32_bf16 v[16:19], v[104:107], v[32:35], v[16:19]\n\t"
      "s_waitcnt lgkmcnt(6)\n\t"
      "v_mfma_f32_16x16x32_bf16 v[12:15], v[68:71], v[36:39], v[12:15]\n\t"
      "ds_read_b128 v[32:35], v0 offset:1344\n\t"
      "v_mfma_f32_16x16x32_bf16 v[16:19], v[100:103], v[36:39], v[16:19]\n\t"
      "v_mfma_f32_16x16x32_bf16 v[12:15], v[64:67], v[40:43], v[12:15]\n\t"
      "ds_read_b128 v[36:39], v0 offset:1408\n\t"
      "v_mfma_f32_16x16x32_bf16 v[16:19], v[96:99], v[40:43], v[16:19]\n\t"
      "s_waitcnt lgkmcnt(6)\n\t"
      "v_mfma_f32_16x16x32_bf16 v[12:15], v[60:63], v[44:47], v[12:15]\n\t"
      "ds_read_b128 v[40:43], v0 offset:1472\n\t"
      "v_mfma_f32_16x16x32_bf16 v[16:19], v[92:95], v[44:47], v[16:19]\n\t"
      "v_mfma_f32_16x16x32_bf16 v[12:15], v[56:59], v[48:51], v[12:15]\n\t"
      "ds_read_b128 v[44:47], v0 offset:1536\n\t"
      "v_mfma_f32_16x16x32_bf16 v[16:19], v[88:91], v[48:51], v[16:19]\n\t"
      "v_mfma_f32_16x16x32_bf16 v[8:11], v[120:123], v[48:51], 0\n\t"
      "ds_read_b32 v7, v126 offset:0\n\t"
      "s_nop 3\n\t"
      "v_cvt_pk_bf16_f32 v12, v12, v13\n\t"
      "v_cvt_pk_bf16_f32 v13, v14, v15\n\t"
      "ds_write_b64 v1, v[12:13] offset:512\n\t"
      "ds_add_u32 v5, v6 offset:4\n\t"
      "s_waitcnt lgkmcnt(9)\n\t"
      "v_mfma_f32_16x16x32_bf16 v[16:19], v[84:87], v[52:55], v[16:19]\n\t"
      "s_waitcnt lgkmcnt(2)\n\t"
      "v_readfirstlane_b32 %[t0], v7\n\t"
      "s_setprio 0\n\t"
      "s_and_b32 %[t1], %[t0], 63\n\t"
      "s_cmp_lt_u32 %[t1], %[full]\n\t"
      "s_cbranch_scc1 81f\n\t"
      "s_setprio 2\n\t"
      "81:\n\t"
      "v_mfma_f32_16x16x32_bf16 v[8:11], v[116:119], v[52:55], v[8:11]\n\t"
      "ds_read_b128 v[48:51], v0 offset:1600\n\t"
      "v_mfma_f32_16x16x32_bf16 v[16:19], v[80:83], v[20:23], v[16:19]\n\t"
      "s_andn2_b32 %[t1], %[fm], %[t0]\n\t"
      "s_cbranch_scc1 20f\n\t"
      "21:\n\t"
      "v_mfma_f32_16x16x32_bf16 v[8:11], v[112:115], v[20:23], v[8:11]\n\t"
      "ds_read_b128 v[52:55], v2 offset:0\n\t"
      "v_mfma_f32_16x16x32_bf16 v[16:19], v[76:79], v[24:27], v[16:19]\n\t"
      "ds_read_b128 v[20:23], v0 offset:1664\n\t"
      "v_mfma_f32_16x16x32_bf16 v[8:11], v[108:111], v[24:27], v[8:11]\n\t"
      "v_mfma_f32_16x16x32_bf16 v[16:19], v[72:75], v[28:31], v[16:19]\n\t"
      "ds_read_b128 v[24:27], v0 offset:1728\n\t"
      "v_mfma_f32_16x16x32_bf16 v[8:11], v[104:107], v[28:31], v[8:11]\n\t"
      "v_mfma_f32_16x16x32_bf16 v[16:19], v[68:71], v[32:35], v[16:19]\n\t"
      "ds_read_b128 v[28:31], v0 offset:1792\n\t"
      "v_mfma_f32_16x16x32_bf16 v[8:11], v[100:103], v[32:35], v[8:11]\n\t"
      "v_mfma_f32_16x16x32_bf16 v[16:19], v[64:67], v[36:39], v[16:19]\n\t"
      "ds_read_b128 v[32:35], v0 offset:1856\n\t"
      "v_mfma_f32_16x16x32_bf16 v[8:11], v[96:99], v[36:39], v[8:11]\n\t"
      "v_mfma_f32_16x16x32_bf16 v[16:19], v[60:63], v[40:43], v[16:19]\n\t"
      "ds_read_b128 v[36:39], v0 offset:1920\n\t"
      "v_mfma_f32_16x16x32_bf16 v[8:11], v[92:95], v[40:43], v[8:11]\n\t"
      "v_mfma_f32_16x16x32_bf16 v[16:19], v[56:59], v[44:47], v[16:19]\n\t"
      "ds_read_b128 v[40:43], v0 offset:1984\n\t"
      "v_mfma_f32_16x16x32_bf16 v[8:11], v[88:91], v[44:47], v[8:11]\n\t"
      "v_mfma_f32_16x16x32_bf16 v[12:15], v[120:123], v[44:47], 0\n\t"
      "s_waitcnt lgkmcnt(6)\n\t"
      "v_mfma_f32_16x16x32_bf16 v[8:11], v[84:87], v[48:51], v[8:11]\n\t"
      "ds_read_b128 v[44:47], v0 offset:2048\n\t"
      "v_mfma_f32_16x16x32_bf16 v[12:15], v[116:119], v[48:51], v[12:15]\n\t"
      "s_waitcnt lgkmcnt(5)\n\t"
      "v_mfma_f32_16x16x32_bf16 v[8:11], v[80:83], v[20:23], v[8:11]\n\t"
      "ds_read_b32 v7, v126 offset:4\n\t"
      "v_mfma_f32_16x16x32_bf16 v[12:15], v[112:115], v[20:23], v[12:15]\n\t"
      "v_add_f32 v16, v16, v52\n\t"
      "v_mfma_f32_16x16x32_bf16 v[8:11], v[76:79], v[24:27], v[8:11]\n\t"
      "v_add_f32 v17, v17, v53\n\t"
      "v_add_f32 v18, v18, v54\n\t"
      "v_mfma_f32_16x16x32_bf16 v[12:15], v[108:111], v[24:27], v[12:15]\n\t"
      "v_add_f32 v19, v19, v55\n\t"
      "v_cvt_pk_bf16_f32 v16, v16, v17\n\t"
      "s_waitcnt lgkmcnt(5)\n\t"
      "v_mfma_f32_16x16x32_bf16 v[8:11], v[72:75], v[28:31], v[8:11]\n\t"
      "v_cvt_pk_bf16_f32 v17, v18, v19\n\t"
      "v_mfma_f32_16x16x32_bf16 v[12:15], v[104:107], v[28:31], v[12:15]\n\t"
      "ds_write_b64 v1, v[16:17] offset:1024\n\t"
      "s_waitcnt lgkmcnt(5)\n\t"
      "v_mfma_f32_16x16x32_bf16 v[8:11], v[68:71], v[32:35], v[8:11]\n\t"
      "ds_read_b128 v[48:51], v0 offset:2112\n\t"
      "v_mfma_f32_16x16x32_bf16 v[12:15], v[100:103], v[32:35], v[12:15]\n\t"
      "ds_add_u32 v5, v6 offset:8\n\t"
      "s_waitcnt lgkmcnt(5)\n\t"
      "v_mfma_f32_16x16x32_bf16 v[8:11], v[64:67], v[36:39], v[8:11]\n\t"
      "s_waitcnt lgkmcnt(3)\n\t"
      "v_readfirstlane_b32 %[t0], v7\n\t"
      "s_setprio 0\n\t"
      "s_and_b32 %[t1], %[t0], 63\n\t"
      "s_cmp_lt_u32 %[t1], %[full]\n\t"
      "s_cbranch_scc1 82f\n\t"
      "s_setprio 2\n\t"
      "82:\n\t"
      "v_mfma_f32_16x16x32_bf16 v[12:15], v[96:99], v[36:39], v[12:15]\n\t"
      "s_andn2_b32 %[t1], %[fm], %[t0]\n\t"
      "s_cbranch_scc1 30f\n\t"
      "31:\n\t"
      "v_mfma_f32_16x16x32_bf16 v[8:11], v[60:63], v[40:43], v[8:11]\n\t"
      "ds_read_b128 v[20:23], v0 offset:2176\n\t"
      "v_mfma_f32_16x16x32_bf16 v[12:15], v[92:95], v[40:43], v[12:15]\n\t"
      "ds_read_b128 v[52:55], v2 offset:1024\n\t"
      "v_mfma_f32_16x16x32_bf16 v[8:11], v[56:59], v[44:47], v[8:11]\n\t"
      "ds_read_b128 v[24:27], v0 offset:2240\n\t"
      "v_mfma_f32_16x16x32_bf16 v[12:15], v[88:91], v[44:47], v[12:15]\n\t"
      "ds_read_b128 v[28:31], v0 offset:2304\n\t"
      "v_mfma_f32_16x16x32_bf16 v[16:19], v[120:123], v[44:47], 0\n\t"
      "ds_read_b128 v[32:35], v0 offset:2368\n\t"
      "s_waitcnt lgkmcnt(5)\n\t"
      "v_mfma_f32_16x16x32_bf16 v[12:15], v[84:87], v[48:51], v[12:15]\n\t"
      "ds_read_b128 v[36:39], v0 offset:2432\n\t"
      "v_mfma_f32_16x16x32_bf16 v[16:19], v[116:119], v[48:51], v[16:19]\n\t"
      "ds_read_b128 v[40:43], v0 offset:2496\n\t"
      "s_waitcnt lgkmcnt(5)\n\t"
      "v_mfma_f32_16x16x32_bf16 v[12:15], v[80:83], v[20:23], v[12:15]\n\t"
      "ds_read_b32 v7, v126 offset:8\n\t"
      "v_mfma_f32_16x16x32_bf16 v[16:19], v[112:115], v[20:23], v[16:19]\n\t"
      "v_add_f32 v8, v8, v52\n\t"
      "s_waitcnt lgkmcnt(5)\n\t"
      "v_mfma_f32_16x16x32_bf16 v[12:15], v[76:79], v[24:27], v[12:15]\n\t"
      "v_add_f32 v9, v9, v53\n\t"
      "v_add_f32 v10, v10, v54\n\t"
      "v_mfma_f32_16x16x32_bf16 v[16:19], v[108:111], v[24:27], v[16:19]\n\t"
      "v_add_f32 v11, v11, v55\n\t"
      "v_cvt_pk_bf16_f32 v8, v8, v9\n\t"
      "s_waitcnt lgkmcnt(4)\n\t"
      "v_mfma_f32_16x16x32_bf16 v[12:15], v[72:75], v[28:31], v[12:15]\n\t"
      "ds_read_b128 v[44:47], v0 offset:2560\n\t"
      "v_cvt_pk_bf16_f32 v9, v10, v11\n\t"
      "v_mfma_f32_16x16x32_bf16 v[16:19], v[104:107], v[28:31], v[16:19]\n\t"
      "ds_write_b64 v1, v[8:9] offset:1536\n\t"
      "s_waitcnt lgkmcnt(5)\n\t"
      "v_mfma_f32_16x16x32_bf16 v[12:15], v[68:71], v[32:35], v[12:15]\n\t"
      "ds_read_b128 v[48:51], v0 offset:2624\n\t"
      "v_mfma_f32_16x16x32_bf16 v[16:19], v[100:103], v[32:35], v[16:19]\n\t"
      "ds_add_u32 v5, v6 offset:12\n\t"
      "s_waitcnt lgkmcnt(5)\n\t"
      "v_mfma_f32_16x16x32_bf16 v[12:15], v[64:67], v[36:39], v[12:15]\n\t"
      "s_waitcnt lgkmcnt(4)\n\t"
      "v_readfirstlane_b32 %[t0], v7\n\t"
      "s_setprio 0\n\t"
      "s_and_b32 %[t1], %[t0], 63\n\t"
      "s_cmp_lt_u32 %[t1], %[full]\n\t"
      "s_cbranch_scc1 83f\n\t"
      "s_setprio 2\n\t"
      "83:\n\t"
      "v_mfma_f32_16x16x32_bf16 v[16:19], v[96:99], v[36:39], v[16:19]\n\t"
      "s_andn2_b32 %[t1], %[fm], %[t0]\n\t"
      "s_cbranch_scc1 40f\n\t"
      "41:\n\t"
      "v_mfma_f32_16x16x32_bf16 v[12:15], v[60:63], v[40:43], v[12:15]\n\t"
      "ds_read_b128 v[20:23], v0 offset:2688\n\t"
      "v_mfma_f32_16x16x32_bf16 v[16:19], v[92:95], v[40:43], v[16:19]\n\t"
      "ds_read_b128 v[52:55], v2 offset:2048\n\t"
      "s_waitcnt lgkmcnt(5)\n\t"
      "v_mfma_f32_16x16x32_bf16 v[12:15], v[56:59], v[44:47], v[12:15]\n\t"
      "ds_read_b128 v[24:27], v0 offset:2752\n\t"
      "v_mfma_f32_16x16x32_bf16 v[16:19], v[88:91], v[44:47], v[16:19]\n\t"
      "ds_read_b128 v[28:31], v0 offset:2816\n\t"
      "v_mfma_f32_16x16x32_bf16 v[8:11], v[120:123], v[44:47], 0\n\t"
      "ds_read_b128 v[32:35], v0 offset:2880\n\t"
      "s_waitcnt lgkmcnt(5)\n\t"
      "v_mfma_f32_16x16x32_bf16 v[16:19], v[84:87], v[48:51], v[16:19]\n\t"
      "ds_read_b128 v[36:39], v0 offset:2944\n\t"
      "v_mfma_f32_16x16x32_bf16 v[8:11], v[116:119], v[48:51], v[8:11]\n\t"
      "ds_read_b128 v[40:43], v0 offset:3008\n\t"
      "s_waitcnt lgkmcnt(5)\n\t"
      "v_mfma_f32_16x16x32_bf16 v[16:19], v[80:83], v[20:23], v[16:19]\n\t"
      "ds_read_b32 v7, v126 offset:12\n\t"
      "v_mfma_f32_16x16x32_bf16 v[8:11], v[112:115], v[20:23], v[8:11]\n\t"
      "v_add_f32 v12, v12, v52\n\t"
      "s_waitcnt lgkmcnt(5)\n\t"
      "v_mfma_f32_16x16x32_bf16 v[16:19], v[76:79], v[24:27], v[16:19]\n\t"
      "v_add_f32 v13, v13, v53\n\t"
      "v_add_f32 v14, v14, v54\n\t"
      "v_mfma_f32_16x16x32_bf16 v[8:11], v[108:111], v[24:27], v[8:11]\n\t"
      "v_add_f32 v15, v15, v55\n\t"
      "v_cvt_pk_bf16_f32 v12, v12, v13\n\t"
      "s_waitcnt lgkmcnt(4)\n\t"
      "v_mfma_f32_16x16x32_bf16 v[16:19], v[72:75], v[28:31], v[16:19]\n\t"
      "ds_read_b128 v[44:47], v0 offset:3072\n\t"
      "v_cvt_pk_bf16_f32 v13, v14, v15\n\t"
      "v_mfma_f32_16x16x32_bf16 v[8:11], v[104:107], v[28:31], v[8:11]\n\t"
      "ds_write_b64 v1, v[12:13] offset:2048\n\t"
      "s_waitcnt lgkmcnt(5)\n\t"
      "v_mfma_f32_16x16x32_bf16 v[16:19], v[68:71], v[32:35], v[16:19]\n\t"
      "ds_read_b128 v[48:51], v0 offset:3136\n\t"
      "v_mfma_f32_16x16x32_bf16 v[8:11], v[100:103], v[32:35], v[8:11]\n\t"
      "ds_add_u32 v5, v6 offset:16\n\t"
      "s_waitcnt lgkmcnt(5)\n\t"
      "v_mfma_f32_16x16x32_bf16 v[16:19], v[64:67], v[36:39], v[16:19]\n\t"
      "s_waitcnt lgkmcnt(4)\n\t"
      "v_readfirstlane_b32 %[t0], v7\n\t"
      "s_setprio 0\n\t"
      "s_and_b32 %[t1], %[t0], 63\n\t"
      "s_cmp_lt_u32 %[t1], %[full]\n\t"
      "s_cbranch_scc1 84f\n\t"
      "s_setprio 2\n\t"
      "84:\n\t"
      "v_mfma_f32_16x16x32_bf16 v[8:11], v[96:99], v[36:39], v[8:11]\n\t"
      "s_andn2_b32 %[t1], %[fm], %[t0]\n\t"
      "s_cbranch_scc1 50f\n\t"
      "51:\n\t"
      "v_mfma_f32_16x16x32_bf16 v[16:19], v[60:63], v[40:43], v[16:19]\n\t"
      "ds_read_b128 v[20:23], v0 offset:3200\n\t"
      "v_mfma_f32_16x16x32_bf16 v[8:11], v[92:95], v[40:43], v[8:11]\n\t"
      "ds_read_b128 v[52:55], v2 offset:3072\n\t"
      "s_waitcnt lgkmcnt(5)\n\t"
      "v_mfma_f32_16x16x32_bf16 v[16:19], v[56:59], v[44:47], v[16:19]\n\t"
      "ds_read_b128 v[24:27], v0 offset:3264\n\t"
      "v_mfma_f32_16x16x32_bf16 v[8:11], v[88:91], v[44:47], v[8:11]\n\t"
      "ds_read_b128 v[28:31], v0 offset:3328\n\t"
      "v_mfma_f32_16x16x32_bf16 v[12:15], v[120:123], v[44:47], 0\n\t"
      "ds_read_b128 v[32:35], v0 offset:3392\n\t"
      "s_waitcnt lgkmcnt(5)\n\t"
      "v_mfma_f32_16x16x32_bf16 v[8:11], v[84:87], v[48:51], v[8:11]\n\t"
      "ds_read_b128 v[36:39], v0 offset:3456\n\t"
      "v_mfma_f32_16x16x32_bf16 v[12:15], v[116:119], v[48:51], v[12:15]\n\t"
      "ds_read_b128 v[40:43], v0 offset:3520\n\t"
      "s_waitcnt lgkmcnt(5)\n\t"
      "v_mfma_f32_16x16x32_bf16 v[8:11], v[80:83], v[20:23], v[8:11]\n\t"
      "ds_read_b32 v7, v126 offset:16\n\t"
      "v_mfma_f32_16x16x32_bf16 v[12:15], v[112:115], v[20:23], v[12:15]\n\t"
      "v_add_f32 v16, v16, v52\n\t"
      "s_waitcnt lgkmcnt(5)\n\t"
      "v_mfma_f32_16x16x32_bf16 v[8:11], v[76:79], v[24:27], v[8:11]\n\t"
      "v_add_f32 v17, v17, v53\n\t"
      "v_add_f32 v18, v18, v54\n\t"
      "v_mfma_f32_16x16x32_bf16 v[12:15], v[108:111], v[24:27], v[12:15]\n\t"
      "v_add_f32 v19, v19, v55\n\t"
      "v_cvt_pk_bf16_f32 v16, v16, v17\n\t"
      "s_waitcnt lgkmcnt(4)\n\t"
      "v_mfma_f32_16x16x32_bf16 v[8:11], v[72:75], v[28:31], v[8:11]\n\t"
      "ds_read_b128 v[44:47], v0 offset:3584\n\t"
      "v_cvt_pk_bf16_f32 v17, v18, v19\n\t"
      "v_mfma_f32_16x16x32_bf16 v[12:15], v[104:107], v[28:31], v[12:15]\n\t"
      "ds_write_b64 v1, v[16:17] offset:2560\n\t"
      "s_waitcnt lgkmcnt(5)\n\t"
      "v_mfma_f32_16x16x32_bf16 v[8:11], v[68:71], v[32:35], v[8:11]\n\t"
      "ds_read_b128 v[48:51], v0 offset:3648\n\t"
      "v_mfma_f32_16x16x32_bf16 v[12:15], v[100:103], v[32:35], v[12:15]\n\t"
      "ds_add_u32 v5, v6 offset:20\n\t"
      "s_waitcnt lgkmcnt(5)\n\t"
      "v_mfma_f32_16x16x32_bf16 v[8:11], v[64:67], v[36:39], v[8:11]\n\t"
      "s_waitcnt lgkmcnt(4)\n\t"
      "v_readfirstlane_b32 %[t0], v7\n\t"
      "s_setprio 0\n\t"
      "s_and_b32 %[t1], %[t0], 63\n\t"
      "s_cmp_lt_u32 %[t1], %[full]\n\t"
      "s_cbranch_scc1 85f\n\t"
      "s_setprio 2\n\t"
      "85:\n\t"
      "v_mfma_f32_16x16x32_bf16 v[12:15], v[96:99], v[36:39], v[12:15]\n\t"
      "s_andn2_b32 %[t1], %[fb], %[t0]\n\t"
      "s_cbranch_scc1 60f\n\t"
      "61:\n\t"
      "v_mfma_f32_16x16x32_bf16 v[8:11], v[60:63], v[40:43], v[8:11]\n\t"
      "ds_read_b128 v[20:23], v0 offset:3712\n\t"
      "v_mfma_f32_16x16x32_bf16 v[12:15], v[92:95], v[40:43], v[12:15]\n\t"
      "ds_read_b128 v[52:55], v2 offset:0\n\t"
      "s_waitcnt lgkmcnt(5)\n\t"
      "v_mfma_f32_16x16x32_bf16 v[8:11], v[56:59], v[44:47], v[8:11]\n\t"
      "ds_read_b128 v[24:27], v0 offset:3776\n\t"
      "v_mfma_f32_16x16x32_bf16 v[12:15], v[88:91], v[44:47], v[12:15]\n\t"
      "ds_read_b128 v[28:31], v0 offset:3840\n\t"
      "s_waitcnt lgkmcnt(4)\n\t"
      "v_mfma_f32_16x16x32_bf16 v[12:15], v[84:87], v[48:51], v[12:15]\n\t"
      "ds_read_b128 v[32:35], v0 offset:3904\n\t"
      "s_waitcnt lgkmcnt(4)\n\t"
      "v_mfma_f32_16x16x32_bf16 v[12:15], v[80:83], v[20:23], v[12:15]\n\t"
      "ds_read_b128 v[36:39], v0 offset:3968\n\t"
      "s_waitcnt lgkmcnt(3)\n\t"
      "v_mfma_f32_16x16x32_bf16 v[12:15], v[76:79], v[24:27], v[12:15]\n\t"
      "ds_read_b128 v[40:43], v0 offset:4032\n\t"
      "s_waitcnt lgkmcnt(3)\n\t"
      "v_mfma_f32_16x16x32_bf16 v[12:15], v[72:75], v[28:31], v[12:15]\n\t"
      "ds_read_b128 v[44:47], v0 offset:4096\n\t"
      "s_waitcnt lgkmcnt(3)\n\t"
      "v_mfma_f32_16x16x32_bf16 v[12:15], v[68:71], v[32:35], v[12:15]\n\t"
      "ds_read_b32 v7, v126 offset:20\n\t"
      "s_waitcnt lgkmcnt(3)\n\t"
      "v_mfma_f32_16x16x32_bf16 v[12:15], v[64:67], v[36:39], v[12:15]\n\t"
      "s_waitcnt lgkmcnt(0)\n\t"
      "v_readfirstlane_b32 %[t0], v7\n\t"
      "s_nop 1\n\t"
      "s_andn2_b32 %[t1], %[fb], %[t0]\n\t"
      "s_cbranch_scc1 70f\n\t"
      "71:\n\t"
      "v_mfma_f32_16x16x32_bf16 v[12:15], v[60:63], v[40:43], v[12:15]\n\t"
      "ds_read_b128 v[48:51], v2 offset:1024\n\t"
      "v_mfma_f32_16x16x32_bf16 v[12:15], v[56:59], v[44:47], v[12:15]\n\t"
      "v_add_f32 v8, v8, v52\n\t"
      "v_add_f32 v9, v9, v53\n\t"
      "v_add_f32 v10, v10, v54\n\t"
      "v_add_f32 v11, v11, v55\n\t"
      "v_cvt_pk_bf16_f32 v8, v8, v9\n\t"
      "v_cvt_pk_bf16_f32 v9, v10, v11\n\t"
      "ds_write_b64 v1, v[8:9] offset:3072\n\t"
      "ds_add_u32 v5, v6 offset:24\n\t"
      "s_waitcnt lgkmcnt(2)\n\t"
      "v_add_f32 v12, v12, v48\n\t"
      "v_add_f32 v13, v13, v49\n\t"
      "v_add_f32 v14, v14, v50\n\t"
      "v_add_f32 v15, v15, v51\n\t"
      "v_cvt_pk_bf16_f32 v12, v12, v13\n\t"
      "v_cvt_pk_bf16_f32 v13, v14, v15\n\t"
      "ds_write_b64 v1, v[12:13] offset:3584\n\t"
      "ds_add_u32 v5, v6 offset:28\n\t"
      "s_waitcnt lgkmcnt(0)\n\t"
      "s_branch 99f\n\t"
      "140:\n\t"
      "s_sleep 2\n\t"
      "ds_read_b32 v7, v13 offset:60\n\t"
      "s_waitcnt lgkmcnt(0)\n\t"
      "s_sub_u32 %[cnt], %[cnt], 1\n\t"
      "s_cbranch_scc1 141b\n\t"
      "v_cmp_gt_u32 vcc, 4, v7\n\t"
      "s_cbranch_vccnz 140b\n\t"
      "s_branch 141b\n\t"
      "20:\n\t"
      "s_sleep 1\n\t"
      "ds_read_b32 v7, v126 offset:0\n\t"
      "s_waitcnt lgkmcnt(0)\n\t"
      "v_readfirstlane_b32 %[t0], v7\n\t"
      "s_sub_u32 %[cnt], %[cnt], 1\n\t"
      "s_cbranch_scc1 21b\n\t"
      "s_andn2_b32 %[t1], %[fm], %[t0]\n\t"
      "s_cbranch_scc1 20b\n\t"
      "s_branch 21b\n\t"
      "30:\n\t"
      "s_sleep 1\n\t"
      "ds_read_b32 v7, v126 offset:4\n\t"
      "s_waitcnt lgkmcnt(0)\n\t"
      "v_readfirstlane_b32 %[t0], v7\n\t"
      "s_sub_u32 %[cnt], %[cnt], 1\n\t"
      "s_cbranch_scc1 31b\n\t"
      "s_andn2_b32 %[t1], %[fm], %[t0]\n\t"
      "s_cbranch_scc1 30b\n\t"
      "s_branch 31b\n\t"
      "40:\n\t"
      "s_sleep 1\n\t"
      "ds_read_b32 v7, v126 offset:8\n\t"
      "s_waitcnt lgkmcnt(0)\n\t"
      "v_readfirstlane_b32 %[t0], v7\n\t"
      "s_sub_u32 %[cnt], %[cnt], 1\n\t"
      "s_cbranch_scc1 41b\n\t"
      "s_andn2_b32 %[t1], %[fm], %[t0]\n\t"
      "s_cbranch_scc1 40b\n\t"
      "s_branch 41b\n\t"
      "50:\n\t"
      "s_sleep 1\n\t"
      "ds_read_b32 v7, v126 offset:12\n\t"
      "s_waitcnt lgkmcnt(0)\n\t"
      "v_readfirstlane_b32 %[t0], v7\n\t"
      "s_sub_u32 %[cnt], %[cnt], 1\n\t"
      "s_cbranch_scc1 51b\n\t"
      "s_andn2_b32 %[t1], %[fm], %[t0]\n\t"
      "s_cbranch_scc1 50b\n\t"
      "s_branch 51b\n\t"
      "60:\n\t"
      "s_sleep 1\n\t"
      "ds_read_b32 v7, v126 offset:16\n\t"
      "s_waitcnt lgkmcnt(0)\n\t"
      "v_readfirstlane_b32 %[t0], v7\n\t"
      "s_sub_u32 %[cnt], %[cnt], 1\n\t"
      "s_cbranch_scc1 61b\n\t"
      "s_andn2_b32 %[t1], %[fb], %[t0]\n\t"
      "s_cbranch_scc1 60b\n\t"
      "s_branch 61b\n\t"
      "70:\n\t"
      "s_sleep 1\n\t"
      "ds_read_b32 v7, v126 offset:20\n\t"
      "s_waitcnt lgkmcnt(0)\n\t"
      "v_readfirstlane_b32 %[t0], v7\n\t"
      "s_sub_u32 %[cnt], %[cnt], 1\n\t"
      "s_cbranch_scc1 71b\n\t"
      "s_andn2_b32 %[t1], %[fb], %[t0]\n\t"
      "s_cbranch_scc1 70b\n\t"
      "s_branch 71b\n\t"
      "99:\n\t"
      : [cnt] "+s"(cnt), [t0] "=&s"(t0), [t1] "=&s"(t1)
      : [ff] "s"(OFF_FLAG + 32 + 4 * c), [fl] "s"(OFF_FLAG), [bit] "s"(1 << c), [fb] "s"(256 << c), [fm] "s"(0x10000 | 256 << c), [full] "s"((1 << nch) - 1),
        "{v0}"(R[0]), "{v1}"(R[1]), "{v2}"(R[2]), "{v3}"(R[3]), "{v4}"(R[4]), "{v5}"(R[5]), "{v6}"(R[6]), "{v7}"(R[7]), "{v8}"(R[8]), "{v9}"(R[9]), "{v10}"(R[10]), "{v11}"(R[11]), "{v12}"(R[12]), "{v13}"(R[13]), "{v14}"(R[14]), "{v15}"(R[15]), "{v16}"(R[16]), "{v17}"(R[17]), "{v18}"(R[18]), "{v19}"(R[19]), "{v20}"(R[20]), "{v21}"(R[21]), "{v22}"(R[22]), "{v23}"(R[23]), "{v24}"(R[24]), "{v25}"(R[25]), "{v26}"(R[26]), "{v27}"(R[27])
      : "memory", "scc", "vcc", "v28", "v29", "v30", "v31", "v32", "v33", "v34", "v35", "v36", "v37", "v38", "v39", "v40", "v41", "v42", "v43", "v44", "v45", "v46", "v47", "v48", "v49", "v50", "v51", "v52", "v53", "v54", "v55", "v56", "v57", "v58", "v59", "v60", "v61", "v62", "v63", "v64", "v65", "v66", "v67", "v68", "v69", "v70", "v71", "v72", "v73", "v74", "v75", "v76", "v77", "v78", "v79", "v80", "v81", "v82", "v83", "v84", "v85", "v86", "v87", "v88", "v89", "v90", "v91", "v92", "v93", "v94", "v95", "v96", "v97", "v98", "v99", "v100", "v101", "v102", "v103", "v104", "v105", "v106", "v107", "v108", "v109", "v110", "v111", "v112", "v113", "v114", "v115", "v116", "v117", "v118", "v119", "v120", "v121", "v122", "v123", "v124", "v125", "v126");
    ENDPGM;
  } else {
    asm volatile(
      "v_mbcnt_lo_u32_b32 v29, -1, 0\n\t"
      "v_mbcnt_hi_u32_b32 v29, -1, v29\n\t"
      "v_mov_b32 v30, %[ka]\n\t"
      "v_mov_b32 v33, %[pk]\n\t"
      "v_lshl_add_u32 v28, v29, 2, %[yf]\n\t"
      "v_lshl_add_u32 v28, v29, 24, v28\n\t"
      "v_mov_b32 v32, %[fl]\n\t"
      "v_mov_b32 v29, %[bit]\n\t"
      "v_mov_b32 v34, v32\n\t"
      "v_mov_b32 v32, %[yf]\n\t"
      "110:\n\t"
      "ds_read_b32 v31, v32 offset:60\n\t"
      "s_waitcnt lgkmcnt(0)\n\t"
      "v_readfirstlane_b32 %[t0], v31\n\t"
      "s_sub_u32 %[cnt], %[cnt], 1\n\t"
      "s_cbranch_scc1 111f\n\t"
      "s_cmp_ge_u32 %[t0], 3\n\t"
      "s_cbranch_scc1 111f\n\t"
      "s_sleep 2\n\t"
      "s_branch 110b\n\t"
      "111:\n\t"
      "ds_read_b128 v[36:39], v0 offset:496\n\t"
      "ds_read_b128 v[40:43], v0 offset:480\n\t"
      "ds_read_b128 v[44:47], v1 offset:0\n\t"
      "ds_read_b128 v[48:51], v1 offset:1024\n\t"
      "s_waitcnt lgkmcnt(3)\n\t"
      "v_mfma_f32_16x16x32_bf16 v[52:55], v[36:39], v[4:7], 0\n\t"
      "v_mfma_f32_16x16x32_bf16 v[56:59], v[36:39], v[8:11], 0\n\t"
      "v_mfma_f32_16x16x32_bf16 v[60:63], v[36:39], v[12:15], 0\n\t"
      "s_waitcnt lgkmcnt(2)\n\t"
      "v_mfma_f32_16x16x32_bf16 v[64:67], v[40:43], v[8:11], 0\n\t"
      "s_waitcnt lgkmcnt(1)\n\t"
      "v_mfma_f32_16x16x32_bf16 v[44:47], v[36:39], v[44:47], 0\n\t"
      "s_waitcnt lgkmcnt(0)\n\t"
      "v_mfma_f32_16x16x32_bf16 v[48:51], v[40:43], v[48:51], 0\n\t"
      "s_nop 0\n\t"
      "v_cvt_pk_bf16_f32 v36, v52, v56\n\t"
      "v_cvt_pk_bf16_f32 v37, v53, v57\n\t"
      "v_cvt_pk_bf16_f32 v38, v54, v58\n\t"
      "v_cvt_pk_bf16_f32 v39, v55, v59\n\t"
      "v_cvt_pk_bf16_f32 v40, v60, v64\n\t"
      "v_cvt_pk_bf16_f32 v41, v61, v65\n\t"
      "v_cvt_pk_bf16_f32 v42, v62, v66\n\t"
      "v_cvt_pk_bf16_f32 v43, v63, v67\n\t"
      "v_cvt_pk_bf16_f32 v52, v44, v48\n\t"
      "v_cvt_pk_bf16_f32 v53, v45, v49\n\t"
      "v_cvt_pk_bf16_f32 v54, v46, v50\n\t"
      "v_cvt_pk_bf16_f32 v55, v47, v51\n\t"
      "ds_read_b128 v[44:47], v0 offset:1008\n\t"
      "ds_read_b128 v[48:51], v0 offset:992\n\t"
      "ds_read_b128 v[56:59], v1 offset:0\n\t"
      "ds_read_b128 v[60:63], v1 offset:1024\n\t"
      "100:\n\t"
      "ds_read_b32 v31, v34 offset:0\n\t"
      "s_waitcnt lgkmcnt(0)\n\t"
      "v_readfirstlane_b32 %[t0], v31\n\t"
      "s_sub_u32 %[cnt], %[cnt], 1\n\t"
      "s_cbranch_scc1 101f\n\t"
      "s_cmp_ge_u32 %[t0], 3\n\t"
      "s_cbranch_scc1 101f\n\t"
      "s_sleep 1\n\t"
      "s_branch 100b\n\t"
      "101:\n\t"
      "s_setprio 2\n\t"
      "ds_read_b128 v[64:67], v21 offset:0\n\t"
      "ds_read_b128 v[68:71], v21 offset:1024\n\t"
      "ds_read_b128 v[72:75], v21 offset:2048\n\t"
      "ds_read_b64 v[34:35], v20 offset:0\n\t"
      "ds_read_b64 v[76:77], v20 offset:544\n\t"
      "ds_read_b64 v[78:79], v20 offset:1088\n\t"
      "ds_read_b32 v20, v33 offset:0\n\t"
      "ds_read_b32 v21, v33 offset:544\n\t"
      "ds_read_b32 v80, v33 offset:1088\n\t"
      "s_waitcnt lgkmcnt(7)\n\t"
      "v_add_f32 v64, v64, v68\n\t"
      "v_add_f32 v65, v65, v69\n\t"
      "v_add_f32 v66, v66, v70\n\t"
      "v_add_f32 v67, v67, v71\n\t"
      "s_waitcnt lgkmcnt(6)\n\t"
      "v_add_f32 v64, v64, v72\n\t"
      "v_add_f32 v65, v65, v73\n\t"
      "v_add_f32 v66, v66, v74\n\t"
      "v_add_f32 v67, v67, v75\n\t"
      "v_cvt_pk_bf16_f32 v33, v65, v66\n\t"
      "v_mov_b32_dpp v81, v64 wave_shl:1 row_mask:0xf bank_mask:0xf bound_ctrl:0\n\t"
      "v_and_b32 v33, v33, v26\n\t"
      "v_cvt_pk_bf16_f32 v26, v67, v81\n\t"
      "s_waitcnt lgkmcnt(0)\n\t"
      "v_and_b32 v26, v26, v27\n\t"
      "ds_write2st64_b32 v24, v33, v26 offset1:1\n\t"
      "ds_read2_b32 v[64:65], v3 offset0:32 offset1:33\n\t"
      "ds_read2_b32 v[66:67], v3 offset0:34 offset1:35\n\t"
      "ds_read2_b32 v[68:69], v3 offset0:96 offset1:97\n\t"
      "ds_read2_b32 v[70:71], v3 offset0:98 offset1:99\n\t"
      "v_add_f32 v34, v34, v76\n\t"
      "v_add_f32 v35, v35, v77\n\t"
      "v_add_f32 v34, v34, v78\n\t"
      "v_add_f32 v35, v35, v79\n\t"
      "v_add_f32 v20, v20, v21\n\t"
      "v_add_f32 v20, v20, v80\n\t"
      "v_cvt_pk_bf16_f32 v21, v34, v34\n\t"
      "s_nop 0\n\t"
      "v_mov_b32_dpp v20, v34 wave_shl:1 row_mask:0xf bank_mask:0xf\n\t"
      "v_and_b32 v21, v21, v25\n\t"
      "v_cvt_pk_bf16_f32 v33, v35, v20\n\t"
      "ds_write_b32 v23, v21\n\t"
      "ds_write_b32 v22, v33\n\t"
      "s_waitcnt lgkmcnt(4)\n\t"
      "v_mfma_f32_16x16x32_bf16 v[20:23], v[36:39], v[64:67], 0\n\t"
      "s_waitcnt lgkmcnt(2)\n\t"
      "v_mfma_f32_16x16x32_bf16 v[20:23], v[40:43], v[68:71], v[20:23]\n\t"
      "s_nop 7\n\t"
      "ds_write_b128 v2, v[20:23] offset:0\n\t"
      "ds_add_u32 v28, v29 offset:0\n\t"
      "s_setprio 1\n\t"
      "ds_read2_b32 v[20:21], v3 offset0:16 offset1:17\n\t"
      "ds_read2_b32 v[22:23], v3 offset0:18 offset1:19\n\t"
      "ds_read2_b32 v[24:25], v3 offset0:80 offset1:81\n\t"
      "ds_read2_b32 v[26:27], v3 offset0:82 offset1:83\n\t"
      "ds_read_b32 v30, v30\n\t"
      "ds_read2_b32 v[72:73], v3 offset0:0 offset1:1\n\t"
      "ds_read2_b32 v[74:75], v3 offset0:2 offset1:3\n\t"
      "ds_read2_b32 v[76:77], v3 offset0:64 offset1:65\n\t"
      "ds_read2_b32 v[78:79], v3 offset0:66 offset1:67\n\t"
      "ds_read2_b32 v[80:81], v3 offset0:176 offset1:177\n\t"
      "ds_read2_b32 v[82:83], v3 offset0:178 offset1:179\n\t"
      "ds_read2_b32 v[84:85], v3 offset0:160 offset1:161\n\t"
      "ds_read2_b32 v[86:87], v3 offset0:162 offset1:163\n\t"
      "ds_read2_b32 v[88:89], v3 offset0:144 offset1:145\n\t"
      "ds_read2_b32 v[90:91], v3 offset0:146 offset1:147\n\t"
      "ds_read2_b32 v[92:93], v3 offset0:128 offset1:129\n\t"
      "ds_read2_b32 v[94:95], v3 offset0:130 offset1:131\n\t"
      "20:\n\t"
      "ds_read_b32 v31, v32 offset:0\n\t"
      "s_waitcnt lgkmcnt(0)\n\t"
      "v_readfirstlane_b32 %[t0], v31\n\t"
      "s_sub_u32 %[cnt], %[cnt], 1\n\t"
      "s_cbranch_scc1 21f\n\t"
      "s_andn2_b32 %[t0], %[cm], %[t0]\n\t"
      "s_cbranch_scc0 21f\n\t"
      "s_sleep 1\n\t"
      "s_branch 20b\n\t"
      "21:\n\t"
      "s_setprio 1\n\t"
      "v_mfma_f32_16x16x32_bf16 v[96:99], v[44:47], v[4:7], 0\n\t"
      "v_mfma_f32_16x16x32_bf16 v[100:103], v[44:47], v[8:11], 0\n\t"
      "ds_read_b128 v[104:107], v0 offset:0\n\t"
      "v_mfma_f32_16x16x32_bf16 v[108:111], v[44:47], v[12:15], 0\n\t"
      "v_mfma_f32_16x16x32_bf16 v[112:115], v[48:51], v[8:11], 0\n\t"
      "v_mfma_f32_16x16x32_bf16 v[56:59], v[44:47], v[56:59], 0\n\t"
      "v_mfma_f32_16x16x32_bf16 v[60:63], v[48:51], v[60:63], 0\n\t"
      "v_and_b32 v16, v16, v30\n\t"
      "v_and_b32 v17, v17, v30\n\t"
      "v_and_b32 v18, v18, v30\n\t"
      "v_and_b32 v19, v19, v30\n\t"
      "v_mfma_f32_16x16x32_bf16 v[44:47], v[36:39], v[20:23], 0\n\t"
      "v_mfma_f32_16x16x32_bf16 v[44:47], v[40:43], v[24:27], v[44:47]\n\t"
      "v_cvt_pk_bf16_f32 v48, v96, v100\n\t"
      "v_cvt_pk_bf16_f32 v49, v97, v101\n\t"
      "v_mfma_f32_16x16x32_bf16 v[116:119], v[36:39], v[72:75], 0\n\t"
      "v_cvt_pk_bf16_f32 v50, v98, v102\n\t"
      "v_cvt_pk_bf16_f32 v51, v99, v103\n\t"
      "v_mfma_f32_16x16x32_bf16 v[116:119], v[40:43], v[76:79], v[116:119]\n\t"
      "v_cvt_pk_bf16_f32 v36, v108, v112\n\t"
      "v_cvt_pk_bf16_f32 v37, v109, v113\n\t"
      "v_mfma_f32_16x16x32_bf16 v[116:119], v[52:55], v[80:83], v[116:119]\n\t"
      "v_cvt_pk_bf16_f32 v38, v110, v114\n\t"
      "v_cvt_pk_bf16_f32 v39, v111, v115\n\t"
      "v_mfma_f32_16x16x32_bf16 v[40:43], v[52:55], v[84:87], 0\n\t"
      "v_mfma_f32_16x16x32_bf16 v[96:99], v[52:55], v[88:91], 0\n\t"
      "v_mfma_f32_16x16x32_bf16 v[100:103], v[52:55], v[92:95], 0\n\t"
      "s_waitcnt lgkmcnt(0)\n\t"
      "v_mfma_f32_16x16x32_bf16 v[44:47], v[16:19], v[104:107], v[44:47]\n\t"
      "ds_read_b128 v[52:55], v0 offset:1520\n\t"
      "ds_read_b128 v[92:95], v0 offset:1504\n\t"
      "v_mfma_f32_16x16x32_bf16 v[44:47], v[48:51], v[64:67], v[44:47]\n\t"
      "v_cvt_pk_bf16_f32 v104, v56, v60\n\t"
      "v_cvt_pk_bf16_f32 v105, v57, v61\n\t"
      "v_mfma_f32_16x16x32_bf16 v[44:47], v[36:39], v[68:71], v[44:47]\n\t"
      "v_cvt_pk_bf16_f32 v106, v58, v62\n\t"
      "v_cvt_pk_bf16_f32 v107, v59, v63\n\t"
      "ds_read_b128 v[56:59], v1 offset:0\n\t"
      "ds_read_b128 v[60:63], v1 offset:1024\n\t"
      "s_nop 3\n\t"
      "ds_write_b128 v2, v[44:47] offset:1024\n\t"
      "ds_add_u32 v28, v29 offset:4\n\t"
      "30:\n\t"
      "ds_read_b32 v31, v32 offset:4\n\t"
      "s_waitcnt lgkmcnt(0)\n\t"
      "v_readfirstlane_b32 %[t0], v31\n\t"
      "s_sub_u32 %[cnt], %[cnt], 1\n\t"
      "s_cbranch_scc1 31f\n\t"
      "s_andn2_b32 %[t0], %[cm], %[t0]\n\t"
      "s_cbranch_scc0 31f\n\t"
      "s_sleep 1\n\t"
      "s_branch 30b\n\t"
      "31:\n\t"
      "v_mfma_f32_16x16x32_bf16 v[44:47], v[52:55], v[4:7], 0\n\t"
      "v_mfma_f32_16x16x32_bf16 v[108:111], v[52:55], v[8:11], 0\n\t"
      "ds_read_b128 v[112:115], v0 offset:512\n\t"
      "v_mfma_f32_16x16x32_bf16 v[120:123], v[52:55], v[12:15], 0\n\t"
      "v_mfma_f32_16x16x32_bf16 v[124:127], v[92:95], v[8:11], 0\n\t"
      "v_mfma_f32_16x16x32_bf16 v[56:59], v[52:55], v[56:59], 0\n\t"
      "v_mfma_f32_16x16x32_bf16 v[60:63], v[92:95], v[60:63], 0\n\t"
      "v_mfma_f32_16x16x32_bf16 v[116:119], v[48:51], v[20:23], v[116:119]\n\t"
      "v_mfma_f32_16x16x32_bf16 v[116:119], v[36:39], v[24:27], v[116:119]\n\t"
      "s_nop 0\n\t"
      "v_cvt_pk_bf16_f32 v52, v44, v108\n\t"
      "v_cvt_pk_bf16_f32 v53, v45, v109\n\t"
      "v_mfma_f32_16x16x32_bf16 v[40:43], v[48:51], v[72:75], v[40:43]\n\t"
      "v_cvt_pk_bf16_f32 v54, v46, v110\n\t"
      "v_cvt_pk_bf16_f32 v55, v47, v111\n\t"
      "v_mfma_f32_16x16x32_bf16 v[40:43], v[36:39], v[76:79], v[40:43]\n\t"
      "v_cvt_pk_bf16_f32 v36, v120, v124\n\t"
      "v_cvt_pk_bf16_f32 v37, v121, v125\n\t"
      "v_mfma_f32_16x16x32_bf16 v[40:43], v[104:107], v[80:83], v[40:43]\n\t"
      "v_cvt_pk_bf16_f32 v38, v122, v126\n\t"
      "v_cvt_pk_bf16_f32 v39, v123, v127\n\t"
      "v_mfma_f32_16x16x32_bf16 v[96:99], v[104:107], v[84:87], v[96:99]\n\t"
      "v_mfma_f32_16x16x32_bf16 v[100:103], v[104:107], v[88:91], v[100:103]\n\t"
      "s_waitcnt lgkmcnt(0)\n\t"
      "v_mfma_f32_16x16x32_bf16 v[116:119], v[16:19], v[112:115], v[116:119]\n\t"
      "ds_read_b128 v[44:47], v0 offset:2032\n\t"
      "ds_read_b128 v[48:51], v0 offset:2016\n\t"
      "v_mfma_f32_16x16x32_bf16 v[116:119], v[52:55], v[64:67], v[116:119]\n\t"
      "v_cvt_pk_bf16_f32 v88, v56, v60\n\t"
      "v_cvt_pk_bf16_f32 v89, v57, v61\n\t"
      "v_mfma_f32_16x16x32_bf16 v[116:119], v[36:39], v[68:71], v[116:119]\n\t"
      "v_cvt_pk_bf16_f32 v90, v58, v62\n\t"
      "v_cvt_pk_bf16_f32 v91, v59, v63\n\t"
      "ds_read_b128 v[56:59], v1 offset:0\n\t"
      "ds_read_b128 v[60:63], v1 offset:1024\n\t"
      "s_nop 3\n\t"
      "ds_write_b128 v2, v[116:119] offset:2048\n\t"
      "ds_add_u32 v28, v29 offset:8\n\t"
      "40:\n\t"
      "ds_read_b32 v31, v32 offset:8\n\t"
      "s_waitcnt lgkmcnt(0)\n\t"
      "v_readfirstlane_b32 %[t0], v31\n\t"
      "s_sub_u32 %[cnt], %[cnt], 1\n\t"
      "s_cbranch_scc1 41f\n\t"
      "s_andn2_b32 %[t0], %[cm], %[t0]\n\t"
      "s_cbranch_scc0 41f\n\t"
      "s_sleep 1\n\t"
      "s_branch 40b\n\t"
      "41:\n\t"
      "v_mfma_f32_16x16x32_bf16 v[92:95], v[44:47], v[4:7], 0\n\t"
      "v_mfma_f32_16x16x32_bf16 v[104:107], v[44:47], v[8:11], 0\n\t"
      "ds_read_b128 v[108:111], v0 offset:1024\n\t"
      "v_mfma_f32_16x16x32_bf16 v[112:115], v[44:47], v[12:15], 0\n\t"
      "v_mfma_f32_16x16x32_bf16 v[116:119], v[48:51], v[8:11], 0\n\t"
      "v_mfma_f32_16x16x32_bf16 v[56:59], v[44:47], v[56:59], 0\n\t"
      "v_mfma_f32_16x16x32_bf16 v[60:63], v[48:51], v[60:63], 0\n\t"
      "v_mfma_f32_16x16x32_bf16 v[40:43], v[52:55], v[20:23], v[40:43]\n\t"
      "v_mfma_f32_16x16x32_bf16 v[40:43], v[36:39], v[24:27], v[40:43]\n\t"
      "s_nop 0\n\t"
      "v_cvt_pk_bf16_f32 v44, v92, v104\n\t"
      "v_cvt_pk_bf16_f32 v45, v93, v105\n\t"
      "v_mfma_f32_16x16x32_bf16 v[96:99], v[52:55], v[72:75], v[96:99]\n\t"
      "v_cvt_pk_bf16_f32 v46, v94, v106\n\t"
      "v_cvt_pk_bf16_f32 v47, v95, v107\n\t"
      "v_mfma_f32_16x16x32_bf16 v[96:99], v[36:39], v[76:79], v[96:99]\n\t"
      "v_cvt_pk_bf16_f32 v36, v112, v116\n\t"
      "v_cvt_pk_bf16_f32 v37, v113, v117\n\t"
      "v_mfma_f32_16x16x32_bf16 v[96:99], v[88:91], v[80:83], v[96:99]\n\t"
      "v_cvt_pk_bf16_f32 v38, v114, v118\n\t"
      "v_cvt_pk_bf16_f32 v39, v115, v119\n\t"
      "v_mfma_f32_16x16x32_bf16 v[100:103], v[88:91], v[84:87], v[100:103]\n\t"
      "s_waitcnt lgkmcnt(0)\n\t"
      "v_mfma_f32_16x16x32_bf16 v[40:43], v[16:19], v[108:111], v[40:43]\n\t"
      "ds_read_b128 v[48:51], v0 offset:2544\n\t"
      "ds_read_b128 v[52:55], v0 offset:2528\n\t"
      "v_mfma_f32_16x16x32_bf16 v[40:43], v[44:47], v[64:67], v[40:43]\n\t"
      "v_cvt_pk_bf16_f32 v84, v56, v60\n\t"
      "v_cvt_pk_bf16_f32 v85, v57, v61\n\t"
      "v_mfma_f32_16x16x32_bf16 v[40:43], v[36:39], v[68:71], v[40:43]\n\t"
      "v_cvt_pk_bf16_f32 v86, v58, v62\n\t"
      "v_cvt_pk_bf16_f32 v87, v59, v63\n\t"
      "s_nop 5\n\t"
      "ds_write_b128 v2, v[40:43] offset:3072\n\t"
      "ds_add_u32 v28, v29 offset:12\n\t"
      "50:\n\t"
      "ds_read_b32 v31, v32 offset:12\n\t"
      "s_waitcnt lgkmcnt(0)\n\t"
      "v_readfirstlane_b32 %[t0], v31\n\t"
      "s_sub_u32 %[cnt], %[cnt], 1\n\t"
      "s_cbranch_scc1 51f\n\t"
      "s_andn2_b32 %[t0], %[cm], %[t0]\n\t"
      "s_cbranch_scc0 51f\n\t"
      "s_sleep 1\n\t"
      "s_branch 50b\n\t"
      "51:\n\t"
      "v_mfma_f32_16x16x32_bf16 v[40:43], v[48:51], v[4:7], 0\n\t"
      "v_mfma_f32_16x16x32_bf16 v[56:59], v[48:51], v[8:11], 0\n\t"
      "ds_read_b128 v[60:63], v0 offset:1536\n\t"
      "v_mfma_f32_16x16x32_bf16 v[88:91], v[48:51], v[12:15], 0\n\t"
      "v_mfma_f32_16x16x32_bf16 v[48:51], v[52:55], v[8:11], 0\n\t"
      "v_mfma_f32_16x16x32_bf16 v[96:99], v[44:47], v[20:23], v[96:99]\n\t"
      "v_mfma_f32_16x16x32_bf16 v[96:99], v[36:39], v[24:27], v[96:99]\n\t"
      "s_nop 2\n\t"
      "v_cvt_pk_bf16_f32 v52, v40, v56\n\t"
      "v_cvt_pk_bf16_f32 v53, v41, v57\n\t"
      "v_mfma_f32_16x16x32_bf16 v[100:103], v[44:47], v[72:75], v[100:103]\n\t"
      "v_cvt_pk_bf16_f32 v54, v42, v58\n\t"
      "v_cvt_pk_bf16_f32 v55, v43, v59\n\t"
      "v_mfma_f32_16x16x32_bf16 v[100:103], v[36:39], v[76:79], v[100:103]\n\t"
      "v_cvt_pk_bf16_f32 v36, v88, v48\n\t"
      "v_cvt_pk_bf16_f32 v37, v89, v49\n\t"
      "v_mfma_f32_16x16x32_bf16 v[100:103], v[84:87], v[80:83], v[100:103]\n\t"
      "v_cvt_pk_bf16_f32 v38, v90, v50\n\t"
      "v_cvt_pk_bf16_f32 v39, v91, v51\n\t"
      "s_waitcnt lgkmcnt(0)\n\t"
      "v_mfma_f32_16x16x32_bf16 v[96:99], v[16:19], v[60:63], v[96:99]\n\t"
      "ds_read_b128 v[40:43], v0 offset:3056\n\t"
      "ds_read_b128 v[44:47], v0 offset:3040\n\t"
      "v_mfma_f32_16x16x32_bf16 v[96:99], v[52:55], v[64:67], v[96:99]\n\t"
      "v_mfma_f32_16x16x32_bf16 v[96:99], v[36:39], v[68:71], v[96:99]\n\t"
      "s_nop 7\n\t"
      "ds_write_b128 v2, v[96:99] offset:0\n\t"
      "ds_add_u32 v28, v29 offset:16\n\t"
      "60:\n\t"
      "ds_read_b32 v31, v32 offset:16\n\t"
      "s_waitcnt lgkmcnt(0)\n\t"
      "v_readfirstlane_b32 %[t0], v31\n\t"
      "s_sub_u32 %[cnt], %[cnt], 1\n\t"
      "s_cbranch_scc1 61f\n\t"
      "s_andn2_b32 %[t0], %[cb], %[t0]\n\t"
      "s_cbranch_scc0 61f\n\t"
      "s_sleep 1\n\t"
      "s_branch 60b\n\t"
      "61:\n\t"
      "v_mfma_f32_16x16x32_bf16 v[32:35], v[40:43], v[4:7], 0\n\t"
      "v_mfma_f32_16x16x32_bf16 v[4:7], v[40:43], v[8:11], 0\n\t"
      "ds_read_b128 v[48:51], v0 offset:2048\n\t"
      "v_mfma_f32_16x16x32_bf16 v[56:59], v[40:43], v[12:15], 0\n\t"
      "v_mfma_f32_16x16x32_bf16 v[12:15], v[44:47], v[8:11], 0\n\t"
      "v_mfma_f32_16x16x32_bf16 v[100:103], v[52:55], v[20:23], v[100:103]\n\t"
      "v_mfma_f32_16x16x32_bf16 v[100:103], v[36:39], v[24:27], v[100:103]\n\t"
      "s_nop 2\n\t"
      "v_cvt_pk_bf16_f32 v8, v32, v4\n\t"
      "v_cvt_pk_bf16_f32 v9, v33, v5\n\t"
      "s_waitcnt lgkmcnt(0)\n\t"
      "v_mfma_f32_16x16x32_bf16 v[100:103], v[16:19], v[48:51], v[100:103]\n\t"
      "v_cvt_pk_bf16_f32 v10, v34, v6\n\t"
      "v_cvt_pk_bf16_f32 v11, v35, v7\n\t"
      "v_cvt_pk_bf16_f32 v4, v56, v12\n\t"
      "v_cvt_pk_bf16_f32 v5, v57, v13\n\t"
      "v_cvt_pk_bf16_f32 v6, v58, v14\n\t"
      "v_cvt_pk_bf16_f32 v7, v59, v15\n\t"
      "v_mfma_f32_16x16x32_bf16 v[100:103], v[8:11], v[64:67], v[100:103]\n\t"
      "s_nop 0\n\t"
      "v_mfma_f32_16x16x32_bf16 v[100:103], v[4:7], v[68:71], v[100:103]\n\t"
      "s_nop 7\n\t"
      "ds_write_b128 v2, v[100:103] offset:1024\n\t"
      "ds_add_u32 v28, v29 offset:20\n\t"
      "s_waitcnt lgkmcnt(0)\n\t"
      : [cnt] "+s"(cnt), [t0] "=&s"(t0)
      : [fl] "s"(OFF_FLAG + 64 + 4 * c), [bit] "s"(256 << c), [cb] "s"(1 << c), [cm] "s"(0x10000 | 1 << c), [ka] "s"(kn + 7936 + 512 + 80 * 4), [pk] "s"(kn + 7936 + 512), [yf] "s"(OFF_FLAG),
        "{v0}"(R[0]), "{v1}"(R[1]), "{v2}"(R[2]), "{v3}"(R[3]), "{v4}"(R[4]), "{v5}"(R[5]), "{v6}"(R[6]), "{v7}"(R[7]), "{v8}"(R[8]), "{v9}"(R[9]), "{v10}"(R[10]), "{v11}"(R[11]), "{v12}"(R[12]), "{v13}"(R[13]), "{v14}"(R[14]), "{v15}"(R[15]), "{v16}"(R[16]), "{v17}"(R[17]), "{v18}"(R[18]), "{v19}"(R[19]), "{v20}"(R[20]), "{v21}"(R[21]), "{v22}"(R[22]), "{v23}"(R[23]), "{v24}"(R[24]), "{v25}"(R[25]), "{v26}"(R[26]), "{v27}"(R[27])
      : "memory", "scc", "vcc", "v28", "v29", "v30", "v31", "v32", "v33", "v34", "v35", "v36", "v37", "v38", "v39", "v40", "v41", "v42", "v43", "v44", "v45", "v46", "v47", "v48", "v49", "v50", "v51", "v52", "v53", "v54", "v55", "v56", "v57", "v58", "v59", "v60", "v61", "v62", "v63", "v64", "v65", "v66", "v67", "v68", "v69", "v70", "v71", "v72", "v73", "v74", "v75", "v76", "v77", "v78", "v79", "v80", "v81", "v82", "v83", "v84", "v85", "v86", "v87", "v88", "v89", "v90", "v91", "v92", "v93", "v94", "v95", "v96", "v97", "v98", "v99", "v100", "v101", "v102", "v103", "v104", "v105", "v106", "v107", "v108", "v109", "v110", "v111", "v112", "v113", "v114", "v115", "v116", "v117", "v118", "v119", "v120", "v121", "v122", "v123", "v124", "v125", "v126", "v127");
    ENDPGM;
  }
}

extern "C" void kernel_launch(void* const* d_in, const int* in_sizes, int n_in,
                              void* d_out, int out_size, void* d_ws, size_t ws_size,
                              hipStream_t stream) {
  const char* const* in = (const char* const*)d_in;
  Params p;
  p.b[0] = Br{in[0], in[1], in[2], in[3], in[4], in[3], in[4], in[5], in[6]};
  p.b[1] = Br{in[7], in[8], in[9], in[12], in[13], in[10], in[11], in[14], in[15]};
  p.out = (char*)d_out;
  ssm_fused<<<dim3(8, 32), 1024, 0, stream>>>(p);
}
```
